```python
import torch

def _softmax(x, axis=-1):
    return torch.softmax(x, dim=axis)
def _gelu(x):
    return torch.nn.functional.gelu(x, approximate="tanh")
def _silu(x):
    return torch.nn.functional.silu(x)
def _sigmoid(x):
    return torch.sigmoid(x)
def _layernorm(x, axis=-1, eps=1e-5):
    m = x.mean(dim=axis, keepdim=True)
    v = x.var(dim=axis, keepdim=True, unbiased=False)
    return (x - m) / torch.sqrt(v + eps)
def _scatter_add(tgt, idx, src, axis=0):
    return tgt.clone().index_add_(axis, idx, src)
def _segment_sum(src, seg, n):
    out = torch.zeros((n,) + src.shape[1:], dtype=src.dtype)
    return out.index_add_(0, seg, src)
def _pad(x, pw):
    flat = tuple(v for pair in reversed(pw) for v in pair)
    return torch.nn.functional.pad(x, flat)

kernel_name = 'sequential_preLN__ssm_conv_s4__dense_activation_mish__prompt'

def setup_inputs():

    _w = (lambda a: (lambda t: t.to(torch.bfloat16) if t.is_floating_point() else t)(torch.as_tensor(a)))
    def _A_setup():
        B, L, D, N = 1, 2048, 512, 128
        A_re = -(lambda _x: torch.exp(torch.as_tensor(_x)))((lambda *s: torch.randn(*s, dtype=torch.bfloat16) * 6.4)(D, N))
        A_im = torch.pi * (lambda *s: torch.randn(*s, dtype=torch.bfloat16) * 0.05)(D, N)
        B_re = (lambda *s: torch.randn(*s, dtype=torch.bfloat16) * 0.1)(D, N)
        B_im = (lambda *s: torch.randn(*s, dtype=torch.bfloat16) * 0.1)(D, N)
        C_re = (lambda *s: torch.randn(*s, dtype=torch.bfloat16) * 0.1)(D, N)
        C_im = (lambda *s: torch.randn(*s, dtype=torch.bfloat16) * 0.1)(D, N)
        log_dt = (lambda *s: torch.randn(*s, dtype=torch.bfloat16) * 0.05)(D) * 0.5 - 4.0
        Dskip = (lambda *s: torch.randn(*s, dtype=torch.bfloat16) * 0.05)(D)
        return {
            "u": _w((lambda *s: torch.randn(*s, dtype=torch.bfloat16) * 0.05)(B, L, D)),
            "A_re": _w(A_re),
            "A_im": _w(A_im),
            "B_re": _w(B_re),
            "B_im": _w(B_im),
            "C_re": _w(C_re),
            "C_im": _w(C_im),
            "log_dt": _w(log_dt),
            "Dskip": _w(Dskip),
        }
    _A_in = _A_setup()

    def _B_setup():
        B, S, D, F = 1, 2048, 512, 1792
        return {
            "x": _w((lambda *s: torch.randn(*s, dtype=torch.bfloat16) * 0.05)(B, S, D)),
            "w1": _w((lambda *s: torch.randn(*s, dtype=torch.bfloat16) * 0.8)(D, F) * (D ** -0.5)),
            "b1": _w((lambda *s: torch.randn(*s, dtype=torch.bfloat16) * 0.2)(F)),
            "w2": _w((lambda *s: torch.randn(*s, dtype=torch.bfloat16) * 0.2)(F, D) * (F ** -0.5)),
        }
    _B_in = _B_setup()

    return {
        "A_u": _A_in["u"],
        "A_A_re": _A_in["A_re"],
        "A_A_im": _A_in["A_im"],
        "A_B_re": _A_in["B_re"],
        "A_B_im": _A_in["B_im"],
        "A_C_re": _A_in["C_re"],
        "A_C_im": _A_in["C_im"],
        "A_log_dt": _A_in["log_dt"],
        "A_Dskip": _A_in["Dskip"],
        "B_x": _B_in["x"],
        "B_w1": _B_in["w1"],
        "B_b1": _B_in["b1"],
        "B_w2": _B_in["w2"],
    }

def reference(A_u, A_A_re, A_A_im, A_B_re, A_B_im, A_C_re, A_C_im, A_log_dt, A_Dskip, B_x, B_w1, B_b1, B_w2):
    def _A_ref(u, A_re, A_im, B_re, B_im, C_re, C_im, log_dt, Dskip):
        Bsz, L, D = u.shape
        dt = torch.exp(log_dt)[:, None]
        A = A_re + 1j * A_im
        Bc = B_re + 1j * B_im
        Cc = C_re + 1j * C_im
        logdA = dt * A
        dB = dt * Bc
        l = (lambda x, dt: x.to(dt))(torch.arange(L), A_re.dtype)
        P = torch.exp(logdA[:, :, None] * l[None, None, :])
        K = torch.einsum("dn,dnl,dn->dl", Cc, P, dB).real
        L2 = 2 * L
        Kp = _pad(K, ((0, 0), (0, L)))
        up = (lambda x, *d: x.permute(*d))(u, 0, 2, 1)
        up = _pad(up, ((0, 0), (0, 0), (0, L)))
        Kf = (lambda x, axes=(-2,-1): torch.fft.rfft2(x.float(), dim=axes))(Kp[:, None, :], axes=(-2, -1))
        Uf = (lambda x, axes=(-2,-1): torch.fft.rfft2(x.float(), dim=axes))(up[:, :, None, :], axes=(-2, -1))
        Yf = Uf * Kf[None, :, :, :]
        y = (lambda x, s, axes=(-2,-1): torch.fft.irfft2(x, s=s, dim=axes).to(torch.bfloat16))(Yf, (1, L2), axes=(-2, -1))[:, :, 0, :L]
        y = (lambda x, *d: x.permute(*d))(y, 0, 2, 1) + u * Dskip[None, None, :]
        return y

    def _B_ref(x, w1, b1, w2):
        h = torch.einsum("bsd,df->bsf", x, w1) + b1
        sp = torch.log1p(torch.exp(-torch.abs(h))) + torch.where(h > 0, h, torch.zeros_like(h))
        act = h * torch.tanh(sp)
        return torch.einsum("bsf,fd->bsd", act, w2)

    x = A_u
    h = x + _A_ref(_layernorm(x, axis=-1), A_A_re, A_A_im, A_B_re, A_B_im, A_C_re, A_C_im, A_log_dt, A_Dskip)
    h2 = h + _B_ref(_layernorm(h, axis=-1), B_w1, B_b1, B_w2)
    return h2
```

```cpp
#include <hip/hip_runtime.h>
#include <hip/hip_bf16.h>
#include <stdint.h>
typedef unsigned short u16;
static constexpr int L = 2048, D = 512, N = 128, F = 1792;
__device__ __forceinline__ float bf2f(u16 x) { return __uint_as_float(((unsigned)x) << 16); }
__device__ __forceinline__ u16 f2bf(float x) { unsigned u = __float_as_uint(x); u += 0x7fffu + ((u >> 16) & 1u); return (u16)(u >> 16); }
__device__ __forceinline__ float rbf(float x) { return bf2f(f2bf(x)); }

__global__ void k_ln(const u16* x, u16* u) {
  int row = blockIdx.x; int t = threadIdx.x;
  float v[8]; float s = 0;
  for (int i = 0; i < 8; ++i) { v[i] = bf2f(x[row * D + t * 8 + i]); s += v[i]; }
  for (int o = 32; o; o >>= 1) s += __shfl_xor(s, o);
  float m = s / D; float q = 0;
  for (int i = 0; i < 8; ++i) { float d = v[i] - m; q += d * d; }
  for (int o = 32; o; o >>= 1) q += __shfl_xor(q, o);
  float var = q / D;
  float r = 1.0f / sqrtf(var + 1e-5f);
  for (int i = 0; i < 8; ++i) u[row * D + t * 8 + i] = f2bf((v[i] - m) * r);
}
__global__ void k_kgen(const u16* Are, const u16* Aim, const u16* Bre, const u16* Bim, const u16* Cre, const u16* Cim, const u16* logdt, float* K) {
  int d = blockIdx.x;
  __shared__ float zr[N], zi[N], wr[N], wi[N];
  float dt = rbf(expf(bf2f(logdt[d])));
  if (threadIdx.x < N) {
    int n = threadIdx.x;
    zr[n] = dt * bf2f(Are[d * N + n]); zi[n] = dt * bf2f(Aim[d * N + n]);
    float br = dt * bf2f(Bre[d * N + n]), bi = dt * bf2f(Bim[d * N + n]);
    float cr = bf2f(Cre[d * N + n]), ci = bf2f(Cim[d * N + n]);
    wr[n] = cr * br - ci * bi; wi[n] = cr * bi + ci * br;
  }
  __syncthreads();
  for (int l = threadIdx.x; l < L; l += blockDim.x) {
    float t = rbf((float)l);
    float acc = 0;
    for (int n = 0; n < N; ++n) {
      float e = expf(zr[n] * t); float s, c; sincosf(zi[n] * t, &s, &c);
      acc += e * (wr[n] * c - wi[n] * s);
    }
    K[d * L + l] = acc;
  }
}
__global__ void k_conv(const u16* x, const u16* u, const float* K, const u16* Dskip, u16* h) {
  int d = blockIdx.x;
  __shared__ float us[L], ks[L];
  for (int l = threadIdx.x; l < L; l += blockDim.x) { us[l] = bf2f(u[l * D + d]); ks[l] = K[d * L + l]; }
  __syncthreads();
  float ds = bf2f(Dskip[d]);
  for (int l = threadIdx.x; l < L; l += blockDim.x) {
    float acc = 0;
    for (int m = 0; m <= l; ++m) acc += ks[l - m] * us[m];
    float y = rbf(rbf(acc) + rbf(us[l] * ds));
    h[l * D + d] = f2bf(bf2f(x[l * D + d]) + y);
  }
}
__global__ void k_g1(const u16* a, const u16* w1, const u16* b1, u16* act) {
  int f = blockIdx.x * 64 + (threadIdx.x & 63); int r0 = blockIdx.y * 16 + (threadIdx.x >> 6) * 4;
  float acc[4] = {0, 0, 0, 0};
  for (int k = 0; k < D; ++k) { float w = bf2f(w1[k * F + f]); for (int i = 0; i < 4; ++i) acc[i] += bf2f(a[(r0 + i) * D + k]) * w; }
  for (int i = 0; i < 4; ++i) {
    float hh = acc[i] + bf2f(b1[f]);
    float sp = log1pf(expf(-fabsf(hh))) + fmaxf(hh, 0.f);
    act[(r0 + i) * F + f] = f2bf(hh * tanhf(sp));
  }
}
__global__ void k_g2(const u16* act, const u16* w2, const u16* h, u16* out) {
  int c = blockIdx.x * 64 + (threadIdx.x & 63); int r0 = blockIdx.y * 16 + (threadIdx.x >> 6) * 4;
  float acc[4] = {0, 0, 0, 0};
  for (int k = 0; k < F; ++k) { float w = bf2f(w2[k * D + c]); for (int i = 0; i < 4; ++i) acc[i] += bf2f(act[(r0 + i) * F + k]) * w; }
  for (int i = 0; i < 4; ++i) out[(r0 + i) * D + c] = f2bf(bf2f(h[(r0 + i) * D + c]) + rbf(acc[i]));
}
extern "C" void kernel_launch(void* const* d_in, const int* in_sizes, int n_in, void* d_out, int out_size, void* d_ws, size_t ws_size, hipStream_t stream) {
  const u16* x = (const u16*)d_in[0];
  char* ws = (char*)d_ws;
  u16* u = (u16*)ws; float* K = (float*)(ws + (4 << 20)); u16* h = (u16*)(ws + (8 << 20)); u16* u2 = (u16*)(ws + (12 << 20)); u16* act = (u16*)(ws + (16 << 20));
  k_ln<<<L, 64, 0, stream>>>(x, u);
  k_kgen<<<D, 256, 0, stream>>>((const u16*)d_in[1], (const u16*)d_in[2], (const u16*)d_in[3], (const u16*)d_in[4], (const u16*)d_in[5], (const u16*)d_in[6], (const u16*)d_in[7], K);
  k_conv<<<D, 256, 0, stream>>>(x, u, K, (const u16*)d_in[8], h);
  k_ln<<<L, 64, 0, stream>>>(h, u2);
  k_g1<<<dim3(F / 64, L / 16), 256, 0, stream>>>(u2, (const u16*)d_in[10], (const u16*)d_in[11], act);
  k_g2<<<dim3(D / 64, L / 16), 256, 0, stream>>>(act, (const u16*)d_in[12], h, (u16*)d_out);
}
```

```cpp
#include <hip/hip_runtime.h>
#include <stdint.h>

typedef unsigned short u16;
typedef unsigned int u32;
typedef unsigned long long u64;
using u32x2 = __attribute__((ext_vector_type(2))) unsigned;
using u32x4 = __attribute__((ext_vector_type(4))) unsigned;
using f32x2 = __attribute__((ext_vector_type(2))) float;
using f32x4 = __attribute__((ext_vector_type(4))) float;
using f32x16 = __attribute__((ext_vector_type(16))) float;
using s16x4 = __attribute__((ext_vector_type(4))) short;
using bf16x2 = __attribute__((ext_vector_type(2))) __bf16;
using bf16x8 = __attribute__((ext_vector_type(8))) __bf16;
using h16x2 = __attribute__((ext_vector_type(2))) _Float16;
using h16x8 = __attribute__((ext_vector_type(8))) _Float16;
#define DI __device__ __forceinline__
#define LDSP(T) __attribute__((address_space(3))) T*

static constexpr int SL = 2048, SD = 512, SN = 128, SF = 1792;
static constexpr size_t OFF_XT = 0;
static constexpr size_t OFF_KR = 2u << 20;
static constexpr size_t OFF_HT = 4u << 20;
static constexpr size_t OFF_HROW = 6u << 20;
static constexpr size_t OFF_W1T = 8u << 20;
static constexpr size_t OFF_W2T = 10u << 20;
static constexpr size_t OFF_ACT = 12u << 20;
static constexpr size_t OFF_ST1 = 20u << 20;
static constexpr size_t OFF_CSP = (20u << 20) + 65536;
static constexpr size_t OFF_CB = (20u << 20) + 131072;
#define ST(k, i) do { } while (0)

DI float bflo(u32 w) { return __uint_as_float(w << 16); }
DI float bfhi(u32 w) { return __uint_as_float(w & 0xffff0000u); }
DI float bf2f(u16 x) { return __uint_as_float((u32)x << 16); }
DI u32 pk_bf16(float a, float b) { f32x2 v = {a, b}; return __builtin_bit_cast(u32, __builtin_convertvector(v, bf16x2)); }
DI u32 pk_f16(float a, float b) { f32x2 v = {a, b}; return __builtin_bit_cast(u32, __builtin_convertvector(v, h16x2)); }
DI float rbf(float a) { return bflo(pk_bf16(a, a)); }
DI float dot2bf(u32 a, u32 b, float c) { return __builtin_amdgcn_fdot2_f32_bf16(__builtin_bit_cast(bf16x2, a), __builtin_bit_cast(bf16x2, b), c, false); }
DI f32x16 mfma_bf16(u32x4 a, u32x4 b, f32x16 c) { return __builtin_amdgcn_mfma_f32_32x32x16_bf16(__builtin_bit_cast(bf16x8, a), __builtin_bit_cast(bf16x8, b), c, 0, 0, 0); }
DI f32x16 mfma_f16(u32x4 a, u32x4 b, f32x16 c) { return __builtin_amdgcn_mfma_f32_32x32x16_f16(__builtin_bit_cast(h16x8, a), __builtin_bit_cast(h16x8, b), c, 0, 0, 0); }
template <int CTRL> DI u32 dpp(u32 v) { return __builtin_amdgcn_update_dpp(0u, v, CTRL, 0xf, 0xf, true); }
template <int CTRL> DI float dppf(float v) { return __uint_as_float(dpp<CTRL>(__float_as_uint(v))); }
DI float wave_sum(float v) {
  v += dppf<0x128>(v); v += dppf<0x124>(v); v += dppf<0x122>(v); v += dppf<0x121>(v);
  auto r = __builtin_amdgcn_permlane16_swap(__float_as_uint(v), __float_as_uint(v), false, false);
  v = __uint_as_float(r[0]) + __uint_as_float(r[1]);
  auto q = __builtin_amdgcn_permlane32_swap(__float_as_uint(v), __float_as_uint(v), false, false);
  return __uint_as_float(q[0]) + __uint_as_float(q[1]);
}
DI float half_sum(float v) {
  auto q = __builtin_amdgcn_permlane32_swap(__float_as_uint(v), __float_as_uint(v), false, false);
  return __uint_as_float(q[0]) + __uint_as_float(q[1]);
}
DI u32x2 tr_read(const char* p) { return __builtin_bit_cast(u32x2, __builtin_amdgcn_ds_read_tr16_b64_v4i16((LDSP(s16x4))p)); }
DI u32 fsw(u32 d) { return ((d & 3) << 2) | ((d >> 2) & 3); }

static constexpr int K1_TAB = 0;
static constexpr int K1_PART = 4096;
static constexpr int K1_CMP = 4096 + 28672;
static constexpr int K1_LDS = K1_CMP + 2 * 1296;

__global__ __launch_bounds__(512, 6) void k1(const u16* __restrict__ x, const u16* __restrict__ Are, const u16* __restrict__ Aim,
                                             const u16* __restrict__ Bre, const u16* __restrict__ Bim, const u16* __restrict__ Cre,
                                             const u16* __restrict__ Cim, const u16* __restrict__ logdt, const u16* __restrict__ w1,
                                             char* __restrict__ ws) {
  __shared__ __attribute__((aligned(16))) char smem[K1_LDS];
  const int b = blockIdx.x, tid = threadIdx.x, lane = tid & 63;
  const int w = __builtin_amdgcn_readfirstlane(tid >> 6);
  ST(0, 0);
  if (b < 256) {
    const int d0 = 64 * (b & 7) + 2 * (b >> 3);
    const int d = d0 + (w >> 2), r = lane & 31, h = lane >> 5;
    const int pi = d * SN + (w & 3) * 32 + r;
    const float are = bf2f(Are[pi]), aim = bf2f(Aim[pi]), bre = bf2f(Bre[pi]), bim = bf2f(Bim[pi]), cre = bf2f(Cre[pi]), cim = bf2f(Cim[pi]);
    const float ldt = bf2f(logdt[d]);
    u32 ga[8];
    {
      const int lam0 = 2047 - 8 * (tid & 255);
#pragma unroll
      for (int j = 0; j < 8; j += 2) {
        const int l0 = lam0 - j, l1 = l0 - 1;
        const u32 p = pk_bf16((float)l0, (float)l1);
        const int i0 = l0 < 256 ? l0 : (int)(p & 0xffffu) - (0x4300 - 128);
        const int i1 = l1 < 256 ? l1 : (int)(p >> 16) - (0x4300 - 128);
        ga[j] = K1_CMP + (tid >> 8) * 1296 + 2 * i0;
        ga[j + 1] = K1_CMP + (tid >> 8) * 1296 + 2 * i1;
      }
#pragma unroll
      for (int j = 0; j < 8; ++j) asm volatile("" : "+v"(ga[j]));
    }
    const int tri = r < 8 ? r : r < 12 ? 256 + 2 * (r - 8) : r < 16 ? 512 + 256 * ((r - 12) >> 1) + 4 * (r & 1) : r < 21 ? 1024 + 256 * (r - 16) : 0;
    const float trow = (float)tri, tcol = (float)(8 * r);
    const float dt = rbf(expf(ldt));
    const float a = dt * are * 1.4426950408889634f, bq = dt * aim * 0.15915494309189535f;
    const float dbr = dt * bre, dbi = dt * bim;
    const float wr = cre * dbr - cim * dbi, wi = cre * dbi + cim * dbr;
    u32 mx = __float_as_uint(fmaxf(fabsf(wr), fabsf(wi)) * __builtin_amdgcn_exp2f(fmaxf(a, 0.f) * 2048.f));
    mx = max(mx, dpp<0x128>(mx)); mx = max(mx, dpp<0x124>(mx)); mx = max(mx, dpp<0x122>(mx)); mx = max(mx, dpp<0x121>(mx));
    u32 sb = max((u32)__builtin_amdgcn_readlane(mx, 0), (u32)__builtin_amdgcn_readlane(mx, 16));
    sb = min(max((sb + 0x007fffffu) & 0x7f800000u, 0x00800000u), 0x7e800000u);
    const float scale = __uint_as_float(sb), inv = __uint_as_float(0x7f000000u - sb);
    f32x4* tab = (f32x4*)(smem + K1_TAB + w * 512);
    if (h == 0) tab[r] = f32x4{a, bq, wr * inv, wi * inv};
    __builtin_amdgcn_fence(__ATOMIC_RELEASE, "wavefront");
    __builtin_amdgcn_wave_barrier();
    __builtin_amdgcn_fence(__ATOMIC_ACQUIRE, "wavefront");
    ST(0, 1);
    f32x16 acc = {};
#pragma unroll 1
    for (int st = 0; st < 4; ++st) {
      u32x4 A, B;
#pragma unroll
      for (int j = 0; j < 4; ++j) {
        const f32x4 P = tab[st * 8 + h * 4 + j];
        const f32x2 ab = {P[0], P[1]};
        const f32x2 e1 = ab * tcol, e2 = ab * trow;
        const float m1 = __builtin_amdgcn_exp2f(e1[0]), p1 = __builtin_amdgcn_fractf(e1[1]);
        const float m2 = __builtin_amdgcn_exp2f(e2[0]), p2 = __builtin_amdgcn_fractf(e2[1]);
        const f32x2 f = f32x2{__builtin_amdgcn_cosf(p1), -__builtin_amdgcn_sinf(p1)} * m1;
        const f32x2 g = f32x2{__builtin_amdgcn_cosf(p2), __builtin_amdgcn_sinf(p2)} * m2;
        const f32x2 e = f32x2{P[2], P[3]} * g[0] + f32x2{-P[3], P[2]} * g[1];
        B[j] = pk_f16(f[0], f[1]);
        A[j] = pk_f16(e[0], e[1]);
      }
      acc = mfma_f16(A, B, acc);
    }
    ST(0, 2);
#pragma unroll
    for (int g = 0; g < 3; ++g)
      *(f32x4*)(smem + K1_PART + w * 3584 + r * 112 + g * 32 + h * 16) = f32x4{acc[4 * g], acc[4 * g + 1], acc[4 * g + 2], acc[4 * g + 3]} * scale;
    __syncthreads();
    if (tid < 384) {
      const int c = tid >= 192, t2 = tid - 192 * c, col = t2 & 31, q = t2 >> 5;
      const char* pp = smem + K1_PART + c * 4 * 3584 + col * 112 + q * 16;
      const f32x4 s = *(const f32x4*)pp + *(const f32x4*)(pp + 3584) + *(const f32x4*)(pp + 7168) + *(const f32x4*)(pp + 10752);
      const u32 p0 = pk_bf16(s[0], s[1]), p1 = pk_bf16(s[2], s[3]);
      u16* cmp = (u16*)(smem + K1_CMP + c * 1296);
      if (q < 2) *(u32x2*)(cmp + 8 * col + 4 * q) = u32x2{p0, p1};
      else if (q == 2) *(u32x2*)(cmp + 256 + 4 * col) = u32x2{p0, p1};
      else if (q == 3) { *(u32*)(cmp + 384 + 2 * col) = p0; *(u32*)(cmp + 448 + 2 * col) = p1; }
      else if (q == 4) { cmp[512 + col] = p0; cmp[544 + col] = p0 >> 16; cmp[576 + col] = p1; cmp[608 + col] = p1 >> 16; }
      else if (col == 0) cmp[640] = p0;
    }
    __syncthreads();
    u32x4 o;
#pragma unroll
    for (int j = 0; j < 4; ++j) o[j] = (u32)*(const u16*)(smem + ga[2 * j]) | ((u32)*(const u16*)(smem + ga[2 * j + 1]) << 16);
    *(u32x4*)(ws + OFF_KR + (size_t)(d0 + (tid >> 8)) * 4096 + (tid & 255) * 16) = o;
    ST(0, 3);
  } else if (b < 512) {
    const int rb = b - 256, row = 8 * rb + w;
    const u32x4 v = *(const u32x4*)(x + row * SD + lane * 8);
    *(u32x4*)(smem + w * 1088 + lane * 16) = v;
    float e[8];
#pragma unroll
    for (int j = 0; j < 4; ++j) { e[2 * j] = bflo(v[j]); e[2 * j + 1] = bfhi(v[j]); }
    float s = ((e[0] + e[1]) + (e[2] + e[3])) + ((e[4] + e[5]) + (e[6] + e[7]));
    const float mean = wave_sum(s) * (1.f / SD);
    float q = 0.f;
#pragma unroll
    for (int j = 0; j < 8; ++j) { const float t = e[j] - mean; q += t * t; }
    const float var = wave_sum(q) * (1.f / SD);
    const float sd = __builtin_sqrtf(rbf(rbf(var) + 1e-5f));
    if (lane == 0) ((u32*)(ws + OFF_ST1))[row] = pk_bf16(mean, sd);
    __syncthreads();
    const int i16 = lane & 15;
    const char* p = smem + (i16 >> 2) * 1088 + (64 * w + (lane & 48) + 4 * (i16 & 3)) * 2;
    const u32x2 t0 = tr_read(p), t1 = tr_read(p + 4 * 1088);
    *(u32x4*)(ws + OFF_XT + ((size_t)rb * SD + tid) * 16) = u32x4{t0[0], t0[1], t1[0], t1[1]};
    ST(0, 3);
  } else {
    const int b2 = b - 512, ftp = b2 % 28, dq = b2 / 28;
    const int fp = tid & 31, o = tid >> 5;
    const u32* src = (const u32*)(w1 + (size_t)(128 * dq + 8 * o) * SF + 64 * ftp + 2 * fp);
    u32 rw[8];
#pragma unroll
    for (int j = 0; j < 8; ++j) rw[j] = src[j * (SF / 2)];
    u32x4 f0, f1;
    float s0 = 0.f, s1 = 0.f;
#pragma unroll
    for (int j = 0; j < 4; ++j) {
      f0[j] = __builtin_amdgcn_perm(rw[2 * j + 1], rw[2 * j], 0x05040100u);
      f1[j] = __builtin_amdgcn_perm(rw[2 * j + 1], rw[2 * j], 0x07060302u);
      s0 = dot2bf(f0[j], 0x3f803f80u, s0);
      s1 = dot2bf(f1[j], 0x3f803f80u, s1);
    }
    const int ft = 2 * ftp + (fp >> 4), ks = 8 * dq + (o >> 1);
    u32x4* dst = (u32x4*)(ws + OFF_W1T) + (ft * 32 + ks) * 64 + (o & 1) * 32 + 2 * (fp & 15);
    dst[0] = f0; dst[1] = f1;
    s0 = half_sum(s0); s1 = half_sum(s1);
    if (lane < 32) *(f32x2*)(smem + w * 256 + lane * 8) = f32x2{s0, s1};
    __syncthreads();
    if (tid < 64) {
      float s = 0.f;
#pragma unroll
      for (int j = 0; j < 8; ++j) s += *(const float*)(smem + j * 256 + tid * 4);
      ((float*)(ws + OFF_CSP))[dq * SF + 64 * ftp + tid] = s;
    }
    ST(0, 3);
  }
}

static constexpr int K2_R = 0;
static constexpr int K2_U = 33280;
static constexpr int K2_RED = 48480;
static constexpr int K2_LDS = K2_RED + 16384;

__global__ __launch_bounds__(256, 2) void k2(const u16* __restrict__ dskip, const u16* __restrict__ w2, const u16* __restrict__ b1,
                                             char* __restrict__ ws) {
  __shared__ __attribute__((aligned(16))) char smem[K2_LDS];
  const int b = blockIdx.x, tid = threadIdx.x, lane = tid & 63;
  const int w = __builtin_amdgcn_readfirstlane(tid >> 6);
  ST(1, 0);
  if (b < 256) {
    const int d0 = 64 * (b & 7) + 2 * (b >> 3);
    const int c = w >> 1, hf = w & 1, a = lane & 31, h = lane >> 5, d = d0 + c;
    const u32x4* xt = (const u32x4*)(ws + OFF_XT) + tid * SD + d0;
    const u32x4 xa = xt[0], xb = xt[1];
    const u32x4* stp = (const u32x4*)(ws + OFF_ST1) + 2 * tid;
    const u32x4 s0 = stp[0], s1 = stp[1];
    const char* kp = ws + OFF_KR + (size_t)d0 * 4096 + tid * 16;
    u32x4 ka[2]; u32x2 kb[2];
#pragma unroll
    for (int cc = 0; cc < 2; ++cc) { ka[cc] = *(const u32x4*)(kp + cc * 4096); kb[cc] = *(const u32x2*)(kp + cc * 4096 + 16); }
    const int blk = 32 * hf + a;
    u32x2 xe[4];
#pragma unroll
    for (int g = 0; g < 4; ++g) xe[g] = *(const u32x2*)(ws + OFF_XT + ((size_t)(4 * blk + g) * SD + d) * 16 + 8 * h);
    const float dsk = bf2f(dskip[d]);
    const u32x4 z4 = {0u, 0u, 0u, 0u};
    if (tid < 155) { *(u32x4*)(smem + K2_U + tid * 16) = z4; *(u32x4*)(smem + K2_U + 7600 + tid * 16) = z4; }
    else if (tid >= 224) *(u32x4*)(smem + K2_R + ((tid - 224) >> 2) * 4160 + 4096 + (tid & 3) * 16) = z4;
#pragma unroll
    for (int cc = 0; cc < 2; ++cc) {
      u32 k[6] = {ka[cc][0], ka[cc][1], ka[cc][2], ka[cc][3], tid == 255 ? 0u : kb[cc][0], tid == 255 ? 0u : kb[cc][1]};
      u32 od[5];
#pragma unroll
      for (int j = 0; j < 5; ++j) od[j] = __builtin_amdgcn_alignbit(k[j + 1], k[j], 16);
      char* rp = smem + K2_R + cc * 16640 + tid * 16;
      *(u32x4*)(rp) = u32x4{k[0], k[1], k[2], k[3]};
      *(u32x4*)(rp + 4160) = u32x4{od[0], od[1], od[2], od[3]};
      *(u32x4*)(rp + 8320) = u32x4{k[1], k[2], k[3], k[4]};
      *(u32x4*)(rp + 12480) = u32x4{od[1], od[2], od[3], od[4]};
    }
    {
      float mu[8], ri[8];
#pragma unroll
      for (int j = 0; j < 4; ++j) {
        mu[j] = bflo(s0[j]); ri[j] = __builtin_amdgcn_rcpf(bfhi(s0[j]));
        mu[4 + j] = bflo(s1[j]); ri[4 + j] = __builtin_amdgcn_rcpf(bfhi(s1[j]));
      }
      u32x4 ua, ub;
#pragma unroll
      for (int j = 0; j < 4; ++j) {
        ua[j] = pk_bf16((bflo(xa[j]) - mu[2 * j]) * ri[2 * j], (bfhi(xa[j]) - mu[2 * j + 1]) * ri[2 * j + 1]);
        ub[j] = pk_bf16((bflo(xb[j]) - mu[2 * j]) * ri[2 * j], (bfhi(xb[j]) - mu[2 * j + 1]) * ri[2 * j + 1]);
      }
      char* up = smem + K2_U + (31 + (tid >> 2)) * 80 + (tid & 3) * 16;
      *(u32x4*)up = ua;
      *(u32x4*)(up + 7600) = ub;
    }
    ST(1, 1);
    __syncthreads();
    ST(1, 2);
    const int cc = (3 - a) & 3;
    const char* gp = smem + K2_R + c * 16640 + cc * 4160 + 2 * (2047 - a + 8 * h - cc) - 32 * (32 * hf + 30) - 2048;
    const char* up = smem + K2_U + c * 7600 + (16 + a - 16 * hf) * 80 + 16 * h;
    f32x16 acc0 = {}, acc1 = {};
#pragma unroll
    for (int dd = 0; dd < 16; ++dd) {
#pragma unroll
      for (int s = 0; s < 2; ++s) {
        const int og = 32 * (30 - 2 * dd + s), ou = 80 * (15 - dd) + 32 * s;
        const u32x2 l0 = *(const u32x2*)(gp + 2048 + og), l1 = *(const u32x2*)(gp + 2048 + og + 8);
        const u32x2 h0 = *(const u32x2*)(gp + og), h1 = *(const u32x2*)(gp + og + 8);
        const u32x4 b0 = *(const u32x4*)(up + ou), b1v = *(const u32x4*)(up + ou + 2560);
        const u32x4 glo = {l0[0], l0[1], l1[0], l1[1]}, ghi = {h0[0], h0[1], h1[0], h1[1]};
        acc1 = mfma_bf16(glo, b1v, acc1);
        acc0 = mfma_bf16(glo, b0, acc0);
        acc1 = mfma_bf16(ghi, b0, acc1);
      }
    }
    ST(1, 3);
    f32x16 keep;
    char* rp = smem + K2_RED + w * 4096 + lane * 16;
    if (hf == 0) {
#pragma unroll
      for (int g = 0; g < 4; ++g) *(f32x4*)(rp + g * 1024) = f32x4{acc1[4 * g], acc1[4 * g + 1], acc1[4 * g + 2], acc1[4 * g + 3]};
      keep = acc0;
    } else {
#pragma unroll
      for (int g = 0; g < 4; ++g) *(f32x4*)(rp + g * 1024) = f32x4{acc0[4 * g], acc0[4 * g + 1], acc0[4 * g + 2], acc0[4 * g + 3]};
      keep = acc1;
    }
    const char* ue = smem + K2_U + c * 7600 + (31 + blk) * 80 + 8 * h;
#pragma unroll
    for (int g = 0; g < 4; ++g) {
      const u32x2 uu = *(const u32x2*)(ue + 16 * g);
      keep[4 * g] += bflo(xe[g][0]) + dsk * bflo(uu[0]);
      keep[4 * g + 1] += bfhi(xe[g][0]) + dsk * bfhi(uu[0]);
      keep[4 * g + 2] += bflo(xe[g][1]) + dsk * bflo(uu[1]);
      keep[4 * g + 3] += bfhi(xe[g][1]) + dsk * bfhi(uu[1]);
    }
    __syncthreads();
    const char* pp = smem + K2_RED + (w ^ 1) * 4096 + lane * 16;
    u32x2 o2[4];
#pragma unroll
    for (int g = 0; g < 4; ++g) {
      const f32x4 t = *(const f32x4*)(pp + g * 1024);
      o2[g] = u32x2{pk_bf16(keep[4 * g] + t[0], keep[4 * g + 1] + t[1]), pk_bf16(keep[4 * g + 2] + t[2], keep[4 * g + 3] + t[3])};
    }
    char* hp = ws + OFF_HT + (size_t)((blk >> 2) * SD + d) * 256;
#pragma unroll
    for (int k = 0; k < 4; k += 2) {
      auto r0 = __builtin_amdgcn_permlane32_swap(o2[k][0], o2[k + 1][0], false, false);
      auto r1 = __builtin_amdgcn_permlane32_swap(o2[k][1], o2[k + 1][1], false, false);
      const u32 ch = (blk & 3) * 4 + k + h;
      *(u32x4*)(hp + 16 * (ch ^ fsw(d))) = u32x4{r0[0], r1[0], r0[1], r1[1]};
    }
    ST(1, 4);
  } else {
    const int hb = b - 256, dtp = hb & 7, kg = hb >> 3;
    const int dp = tid & 31, o = tid >> 5, ks = 4 * kg + (o >> 1), hh = o & 1;
    const u32* src = (const u32*)(w2 + (size_t)(16 * ks + 4 * hh) * SD + 64 * dtp + 2 * dp);
    u32 rw[8];
#pragma unroll
    for (int j = 0; j < 8; ++j) rw[j] = src[(8 * (j >> 2) + (j & 3)) * (SD / 2)];
    const u32* pf = (const u32*)(ws + OFF_W1T + (size_t)kg * 65536) + tid * 32;
    const u32 pf0 = pf[0], pf1 = pf[8192];
    if (hb < 7) {
      const int f = hb * 256 + tid;
      const float* cp = (const float*)(ws + OFF_CSP) + f;
      ((f32x2*)(ws + OFF_CB))[f] = f32x2{(cp[0] + cp[SF]) + (cp[2 * SF] + cp[3 * SF]), bf2f(b1[f])};
    }
    u32x4 f0, f1;
#pragma unroll
    for (int j = 0; j < 4; ++j) {
      f0[j] = __builtin_amdgcn_perm(rw[2 * j + 1], rw[2 * j], 0x05040100u);
      f1[j] = __builtin_amdgcn_perm(rw[2 * j + 1], rw[2 * j], 0x07060302u);
    }
    u32x4* dst = (u32x4*)(ws + OFF_W2T) + ((dtp * 112 + ks) * 2 + (dp >> 4)) * 64 + hh * 32 + 2 * (dp & 15);
    dst[0] = f0; dst[1] = f1;
    asm volatile("" :: "v"(pf0), "v"(pf1));
    ST(1, 4);
  }
}

static constexpr int K3_ST = 131072;
static constexpr int K3_LDS = K3_ST + 1024;

__global__ __launch_bounds__(256, 1) void k3(char* __restrict__ ws) {
  __shared__ __attribute__((aligned(16))) char smem[K3_LDS];
  const int b = blockIdx.x, tid = threadIdx.x, lane = tid & 63;
  const int w = __builtin_amdgcn_readfirstlane(tid >> 6);
  const int xcd = b & 7, i = b >> 3;
  ST(2, 0);
  if (i >= 28) {
    const u32* pf = (const u32*)(ws + OFF_W2T + (size_t)(i - 28) * 458752) + tid * 32;
    u32 v[14];
#pragma unroll
    for (int j = 0; j < 14; ++j) v[j] = pf[j * 8192];
#pragma unroll
    for (int j = 0; j < 14; ++j) asm volatile("" :: "v"(v[j]));
    ST(2, 4);
    return;
  }
  const int p = 2 * xcd + (i >= 14), g14 = i >= 14 ? i - 14 : i;
  const int ft = 4 * g14 + w, h = lane >> 5;
  const u32 voff = lane * 16;
  u32x4 cb[8];
  {
    const char* cbp = ws + OFF_CB + ft * 256;
    const u32 vo = h * 32;
#pragma unroll
    for (int g = 0; g < 4; ++g)
      asm volatile("global_load_dwordx4 %0, %2, %3 offset:%4\n\tglobal_load_dwordx4 %1, %2, %3 offset:%5"
                   : "=&v"(cb[2 * g]), "=&v"(cb[2 * g + 1]) : "v"(vo), "s"(cbp), "i"(g * 64), "i"(g * 64 + 16) : "memory");
  }
  u32x4 wf[32];
  const u32 lds0 = (u32)(size_t)(LDSP(char))smem;
#pragma unroll
  for (int st = 0; st < 8; ++st) {
    const char* hsrc = ws + OFF_HT + (size_t)p * 131072 + (4 * st + w) * 4096;
    const u32 dst = lds0 + (4 * st + w) * 4096;
    u32 keep;
    asm volatile("s_mov_b32 %0, m0\n\ts_mov_b32 m0, %3\n\ts_nop 0\n\t"
                 "global_load_lds_dwordx4 %1, %2\n\tglobal_load_lds_dwordx4 %1, %2 offset:1024\n\t"
                 "global_load_lds_dwordx4 %1, %2 offset:2048\n\tglobal_load_lds_dwordx4 %1, %2 offset:3072\n\ts_mov_b32 m0, %0"
                 : "=&s"(keep) : "v"(voff), "s"(hsrc), "s"(dst) : "memory");
    const char* wsrc = ws + OFF_W1T + (size_t)(ft * 32 + 4 * st) * 1024;
    asm volatile("global_load_dwordx4 %0, %4, %5\n\tglobal_load_dwordx4 %1, %4, %5 offset:1024\n\t"
                 "global_load_dwordx4 %2, %4, %5 offset:2048\n\tglobal_load_dwordx4 %3, %4, %5 offset:3072"
                 : "=&v"(wf[4 * st]), "=&v"(wf[4 * st + 1]), "=&v"(wf[4 * st + 2]), "=&v"(wf[4 * st + 3]) : "v"(voff), "s"(wsrc) : "memory");
  }
  const int q = (lane & 15) >> 2, pp = lane & 3, blk = (lane >> 4) & 1;
  const char* ta[4][2];
#pragma unroll
  for (int s = 0; s < 4; ++s)
#pragma unroll
    for (int t = 0; t < 2; ++t) {
      const int m = (s + w) & 3;
      ta[s][t] = smem + 256 * (8 * h + 4 * t + q) + 16 * ((4 * m + 2 * blk + (pp >> 1)) ^ ((q << 2) | (2 * h + t))) + 8 * (pp & 1);
    }
  f32x16 acc[4] = {};
  float s1 = 0.f, s2 = 0.f;
  ST(2, 1);
#pragma unroll
  for (int st = 0; st < 8; ++st) {
    asm volatile("s_waitcnt vmcnt(%4)\n\ts_barrier" : "+v"(wf[4 * st]), "+v"(wf[4 * st + 1]), "+v"(wf[4 * st + 2]), "+v"(wf[4 * st + 3]) : "i"(56 - 8 * st) : "memory");
    u32x4 own[4];
#pragma unroll
    for (int kk = 0; kk < 4; ++kk) {
      const int ks = 4 * st + kk;
#pragma unroll
      for (int s = 0; s < 4; ++s) {
        const u32x2 t0 = tr_read(ta[s][0] + 4096 * ks), t1 = tr_read(ta[s][1] + 4096 * ks);
        const u32x4 bf = {t0[0], t0[1], t1[0], t1[1]};
        acc[s] = mfma_bf16(wf[ks], bf, acc[s]);
        if (s == 0) own[kk] = bf;
      }
    }
    __builtin_amdgcn_sched_barrier(0);
#pragma unroll
    for (int kk = 0; kk < 4; ++kk)
#pragma unroll
      for (int j = 0; j < 4; ++j) { s1 = dot2bf(own[kk][j], 0x3f803f80u, s1); s2 = dot2bf(own[kk][j], own[kk][j], s2); }
    __builtin_amdgcn_sched_barrier(0);
  }
  ST(2, 2);
  s1 = half_sum(s1); s2 = half_sum(s2);
  {
    const float mean = s1 * (1.f / SD);
    const float var = fmaxf(s2 * (1.f / SD) - mean * mean, 0.f);
    const float sd = rbf(__builtin_sqrtf(rbf(rbf(var) + 1e-5f)));
    if (h == 0) *(f32x2*)(smem + K3_ST + (32 * w + lane) * 8) = f32x2{rbf(mean), __builtin_amdgcn_rcpf(sd)};
  }
  {
    char* hr = ws + OFF_HROW + (size_t)(128 * p + 32 * w + (lane & 31)) * 1024 + 16 * h;
    for (int ks = g14; ks < 32; ks += 14) {
      const u32x2 t0 = tr_read(ta[0][0] + 4096 * ks), t1 = tr_read(ta[0][1] + 4096 * ks);
      *(u32x4*)(hr + 32 * ks) = u32x4{t0[0], t0[1], t1[0], t1[1]};
    }
  }
  asm volatile("" : "+v"(cb[0]), "+v"(cb[1]), "+v"(cb[2]), "+v"(cb[3]), "+v"(cb[4]), "+v"(cb[5]), "+v"(cb[6]), "+v"(cb[7]));
  __syncthreads();
  ST(2, 3);
#pragma unroll
  for (int s = 0; s < 4; ++s) {
    const int m = (s + w) & 3;
    const f32x2 sv = *(const f32x2*)(smem + K3_ST + (32 * m + (lane & 31)) * 8);
    u32 o[8];
#pragma unroll
    for (int j = 0; j < 8; ++j) {
      const f32x2 cs = {__uint_as_float(cb[j][0]), __uint_as_float(cb[j][2])}, bb = {__uint_as_float(cb[j][1]), __uint_as_float(cb[j][3])};
      const f32x2 t = f32x2{acc[s][2 * j], acc[s][2 * j + 1]} - cs * sv[0];
      const f32x2 hv = t * sv[1] + bb;
      const f32x2 gv = hv * 1.4426950408889634f;
      const f32x2 pv = f32x2{__builtin_amdgcn_exp2f(gv[0]), __builtin_amdgcn_exp2f(gv[1])} + 1.f;
      const f32x2 dv = pv * pv + 1.f;
      const f32x2 qv = f32x2{__builtin_amdgcn_rcpf(dv[0]), __builtin_amdgcn_rcpf(dv[1])} * -2.f + 1.f;
      const f32x2 rv = hv * qv;
      o[j] = pk_bf16(rv[0], rv[1]);
    }
    u32x4* ap = (u32x4*)(ws + OFF_ACT) + (size_t)(((2 * p + (m >> 1)) * 112 + 2 * ft) * 2 + (m & 1)) * 64 + lane;
    ap[0] = u32x4{o[0], o[1], o[2], o[3]};
    ap[128] = u32x4{o[4], o[5], o[6], o[7]};
  }
  ST(2, 4);
}

static constexpr int K4_DEPTH = 12;
__global__ __launch_bounds__(256, 1) void k4(char* __restrict__ ws, u16* __restrict__ out) {
  __shared__ __attribute__((aligned(16))) char smem[49152];
  const int b = blockIdx.x, tid = threadIdx.x, lane = tid & 63;
  const int w = __builtin_amdgcn_readfirstlane(tid >> 6);
  const int i = b >> 3, ltp = 4 * (b & 7) + (i >> 3), dtp = i & 7, h = lane >> 5;
  ST(3, 0);
  const int row = 64 * ltp + 32 * (w & 1) + (lane & 31), col = 64 * dtp + 32 * (w >> 1) + 8 * h;
  const u32x4* hp = (const u32x4*)(ws + OFF_HROW + (size_t)row * 1024 + col * 2);
  const u32x4 hr0 = hp[0], hr1 = hp[2];
  const u32x4* wp = (const u32x4*)(ws + OFF_W2T) + (size_t)(dtp * 112 + 28 * w) * 128 + lane;
  const u32x4* ap = (const u32x4*)(ws + OFF_ACT) + (size_t)(ltp * 112 + 28 * w) * 128 + lane;
  const u32x4* wp0 = wp + (w >> 1) * 64; const u32x4* wp1 = wp + ((w >> 1) ^ 1) * 64;
  const u32x4* ap0 = ap + (w & 1) * 64; const u32x4* ap1 = ap + ((w & 1) ^ 1) * 64;
  u32x4 wa[28], wb[28], aa[28], ab[28];
#pragma unroll
  for (int k = 0; k < K4_DEPTH; ++k) { wa[k] = wp0[k * 128]; aa[k] = ap0[k * 128]; wb[k] = wp1[k * 128]; ab[k] = ap1[k * 128]; }
  f32x16 acc[4] = {};
#pragma unroll
  for (int k = 0; k < 28; ++k) {
    if (k + K4_DEPTH < 28) { const int n = k + K4_DEPTH; wa[n] = wp0[n * 128]; aa[n] = ap0[n * 128]; wb[n] = wp1[n * 128]; ab[n] = ap1[n * 128]; }
    __builtin_amdgcn_sched_barrier(0);
    acc[0] = mfma_bf16(wa[k], aa[k], acc[0]);
    acc[1] = mfma_bf16(wa[k], ab[k], acc[1]);
    acc[2] = mfma_bf16(wb[k], aa[k], acc[2]);
    acc[3] = mfma_bf16(wb[k], ab[k], acc[3]);
    __builtin_amdgcn_sched_barrier(0);
  }
  ST(3, 2);
#pragma unroll
  for (int s = 1; s < 4; ++s) {
    char* rp = smem + ((w ^ s) * 3 + s - 1) * 4096 + lane * 16;
#pragma unroll
    for (int g = 0; g < 4; ++g) *(f32x4*)(rp + g * 1024) = f32x4{acc[s][4 * g], acc[s][4 * g + 1], acc[s][4 * g + 2], acc[s][4 * g + 3]};
  }
  __syncthreads();
  ST(3, 3);
  f32x4 v[4];
#pragma unroll
  for (int g = 0; g < 4; ++g) {
    const char* rp = smem + w * 3 * 4096 + g * 1024 + lane * 16;
    v[g] = (f32x4{acc[0][4 * g], acc[0][4 * g + 1], acc[0][4 * g + 2], acc[0][4 * g + 3]} + *(const f32x4*)rp) + (*(const f32x4*)(rp + 4096) + *(const f32x4*)(rp + 8192));
  }
  u32x4* op = (u32x4*)(out + (size_t)row * SD + col);
#pragma unroll
  for (int k = 0; k < 4; k += 2) {
    const u32x4 hr = k ? hr1 : hr0;
    float lo[4], hi[4];
#pragma unroll
    for (int e = 0; e < 4; ++e) {
      auto r = __builtin_amdgcn_permlane32_swap(__float_as_uint(v[k][e]), __float_as_uint(v[k + 1][e]), false, false);
      lo[e] = __uint_as_float(r[0]); hi[e] = __uint_as_float(r[1]);
    }
    op[k] = u32x4{pk_bf16(lo[0] + bflo(hr[0]), lo[1] + bfhi(hr[0])), pk_bf16(lo[2] + bflo(hr[1]), lo[3] + bfhi(hr[1])),
                  pk_bf16(hi[0] + bflo(hr[2]), hi[1] + bfhi(hr[2])), pk_bf16(hi[2] + bflo(hr[3]), hi[3] + bfhi(hr[3]))};
  }
  ST(3, 4);
}

extern "C" void kernel_launch(void* const* d_in, const int* in_sizes, int n_in, void* d_out, int out_size, void* d_ws, size_t ws_size,
                              hipStream_t stream) {
  char* ws = (char*)d_ws;
  k1<<<624, 512, 0, stream>>>((const u16*)d_in[0], (const u16*)d_in[1], (const u16*)d_in[2], (const u16*)d_in[3], (const u16*)d_in[4],
                              (const u16*)d_in[5], (const u16*)d_in[6], (const u16*)d_in[7], (const u16*)d_in[10], ws);
  k2<<<480, 256, 0, stream>>>((const u16*)d_in[8], (const u16*)d_in[12], (const u16*)d_in[11], ws);
  k3<<<256, 256, 0, stream>>>(ws);
  k4<<<256, 256, 0, stream>>>(ws, (u16*)d_out);
}
```

```cpp
#include <hip/hip_runtime.h>
#include <stdint.h>

typedef unsigned short u16;
typedef unsigned int u32;
typedef unsigned long long u64;
using u32x2 = __attribute__((ext_vector_type(2))) unsigned;
using u32x4 = __attribute__((ext_vector_type(4))) unsigned;
using f32x2 = __attribute__((ext_vector_type(2))) float;
using f32x4 = __attribute__((ext_vector_type(4))) float;
using f32x16 = __attribute__((ext_vector_type(16))) float;
using s16x4 = __attribute__((ext_vector_type(4))) short;
using bf16x2 = __attribute__((ext_vector_type(2))) __bf16;
using bf16x8 = __attribute__((ext_vector_type(8))) __bf16;
using h16x2 = __attribute__((ext_vector_type(2))) _Float16;
using h16x8 = __attribute__((ext_vector_type(8))) _Float16;
typedef __amdgpu_buffer_rsrc_t rsrc_t;
#define DI __device__ __forceinline__
#define LDSP(T) __attribute__((address_space(3))) T*
#define SB() __builtin_amdgcn_sched_barrier(0)

static constexpr int SL = 2048, SD = 512, SN = 128, SF = 1792;
static constexpr u32 OFF_XT = 0;
static constexpr u32 OFF_HT = 4u << 20;
static constexpr u32 OFF_W1T = 8u << 20;
static constexpr u32 OFF_W2T = 10u << 20;
static constexpr u32 OFF_ACT = 12u << 20;
static constexpr u32 OFF_ST1 = 20u << 20;
static constexpr u32 OFF_CSP = (20u << 20) + 65536;
static constexpr u32 OFF_FA = (21u << 20);
static constexpr u32 OFF_FB = OFF_FA + 32768;
static constexpr u32 OFF_FC = OFF_FB + 32768;
static constexpr u32 OFF_FD = OFF_FC + 32768;
static constexpr u32 OFF_GEN = OFF_FD + 8192;
static constexpr u32 WS_BYTES = 26u << 20;
#define ST(k, i) do { } while (0)

static constexpr int L_R = 0;
static constexpr int L_U = 33280;
static constexpr int L_RED = 48640;
static constexpr int L_TAB = 65536;
static constexpr int L_PART = 69632;
static constexpr int L_CMP = 83968;
static constexpr int L_LN = 86656;
static constexpr int L_S1 = 95360;
static constexpr int L_CS = 95488;
static constexpr int L_H = 0;
static constexpr int L_CB = 131072;
static constexpr int L_SP = 132096;
static constexpr int L_HR = 136192;
static constexpr int L_END = 144384;

DI float bflo(u32 w) { return __uint_as_float(w << 16); }
DI float bfhi(u32 w) { return __uint_as_float(w & 0xffff0000u); }
DI float bf2f(u16 x) { return __uint_as_float((u32)x << 16); }
DI u32 pk_bf16(float a, float b) { f32x2 v = {a, b}; return __builtin_bit_cast(u32, __builtin_convertvector(v, bf16x2)); }
DI u32 pk_f16(float a, float b) { f32x2 v = {a, b}; return __builtin_bit_cast(u32, __builtin_convertvector(v, h16x2)); }
DI float rbf(float a) { return bflo(pk_bf16(a, a)); }
DI float dot2bf(u32 a, u32 b, float c) { return __builtin_amdgcn_fdot2_f32_bf16(__builtin_bit_cast(bf16x2, a), __builtin_bit_cast(bf16x2, b), c, false); }
DI f32x16 mfma_bf16(u32x4 a, u32x4 b, f32x16 c) { return __builtin_amdgcn_mfma_f32_32x32x16_bf16(__builtin_bit_cast(bf16x8, a), __builtin_bit_cast(bf16x8, b), c, 0, 0, 0); }
DI f32x16 mfma_f16(u32x4 a, u32x4 b, f32x16 c) { return __builtin_amdgcn_mfma_f32_32x32x16_f16(__builtin_bit_cast(h16x8, a), __builtin_bit_cast(h16x8, b), c, 0, 0, 0); }
template <int CTRL> DI u32 dpp(u32 v) { return __builtin_amdgcn_update_dpp(0u, v, CTRL, 0xf, 0xf, true); }
template <int CTRL> DI float dppf(float v) { return __uint_as_float(dpp<CTRL>(__float_as_uint(v))); }
DI float half_sum(float v) {
  auto q = __builtin_amdgcn_permlane32_swap(__float_as_uint(v), __float_as_uint(v), false, false);
  return __uint_as_float(q[0]) + __uint_as_float(q[1]);
}
DI float wave_sum(float v) {
  v += dppf<0x128>(v); v += dppf<0x124>(v); v += dppf<0x122>(v); v += dppf<0x121>(v);
  auto r = __builtin_amdgcn_permlane16_swap(__float_as_uint(v), __float_as_uint(v), false, false);
  return half_sum(__uint_as_float(r[0]) + __uint_as_float(r[1]));
}
DI u32 wave_max(u32 v) {
  v = max(v, dpp<0x128>(v)); v = max(v, dpp<0x124>(v)); v = max(v, dpp<0x122>(v)); v = max(v, dpp<0x121>(v));
  auto r = __builtin_amdgcn_permlane16_swap(v, v, false, false);
  v = max(r[0], r[1]);
  auto q = __builtin_amdgcn_permlane32_swap(v, v, false, false);
  return max(q[0], q[1]);
}
DI u32 opq(u32 v) { asm volatile("" : "+v"(v)); return v; }
template <class T> DI T lds_ld(u32 a) { return *(LDSP(const T))(size_t)a; }
template <class T> DI void lds_st(u32 a, T v) { *(LDSP(T))(size_t)a = v; }
DI u32x2 tr_read(u32 a) { return __builtin_bit_cast(u32x2, __builtin_amdgcn_ds_read_tr16_b64_v4i16((LDSP(s16x4))(size_t)a)); }
DI u32x4 cat(u32x2 a, u32x2 b) { return u32x4{a[0], a[1], b[0], b[1]}; }
DI void st16(rsrc_t r, u32 vo, u32 so, u32x4 v) { __builtin_amdgcn_raw_buffer_store_b128(v, r, opq(vo + so), 0, 16); }
DI void st8(rsrc_t r, u32 vo, u32 so, u32x2 v) { __builtin_amdgcn_raw_buffer_store_b64(v, r, vo, so, 16); }
DI void st4(rsrc_t r, u32 vo, u32 so, u32 v) { __builtin_amdgcn_raw_buffer_store_b32(v, r, vo, so, 16); }
DI u32x4 ld16(rsrc_t r, u32 vo, u32 so) { return __builtin_amdgcn_raw_buffer_load_b128(r, vo, so, 16); }
DI u32x2 ld8(rsrc_t r, u32 vo, u32 so) { return __builtin_amdgcn_raw_buffer_load_b64(r, vo, so, 16); }
DI u32 ld4(rsrc_t r, u32 vo, u32 so) { return __builtin_amdgcn_raw_buffer_load_b32(r, vo, so, 16); }
DI void bar() { asm volatile("s_waitcnt lgkmcnt(0)\n\ts_barrier" ::: "memory"); }
DI void bar_vm() { asm volatile("s_waitcnt vmcnt(0) lgkmcnt(0)\n\ts_barrier" ::: "memory"); }

struct Poll { u32x2 v[4]; };
DI Poll poll_issue(rsrc_t r, u32 arr, int b, int lane) {
  Poll p;
#pragma unroll
  for (int k = 0; k < 4; ++k) p.v[k] = ld8(r, lane * 16 + k * 1024, arr + (b & 7) * 4096);
  return p;
}
DI bool poll_ok(const Poll& p, u32x2 tag) {
  bool ok = true;
#pragma unroll
  for (int k = 0; k < 4; ++k) ok = ok && p.v[k][0] == tag[0] && p.v[k][1] == tag[1];
  return __all(ok);
}
DI void poll_wait(Poll p, rsrc_t r, u32 arr, int b, int lane, u32x2 tag) {
#pragma unroll 1
  for (int sp = 0; sp < (1 << 16); ++sp) {
    if (poll_ok(p, tag)) break;
    __builtin_amdgcn_s_sleep(1);
    asm volatile("" ::: "memory");
    p = poll_issue(r, arr, b, lane);
  }
}
DI void flag_all(rsrc_t r, u32 arr, int b, int tid, u32x2 tag) { if (tid < 8) st8(r, tid * 4096 + b * 16, arr, tag); }

#define GLDS4(voff, src, dst)                                                                                             \
  asm volatile("s_mov_b32 m0, %2\n\ts_nop 0\n\tglobal_load_lds_dwordx4 %0, %1 sc1\n\tglobal_load_lds_dwordx4 %0, %1 offset:1024 sc1\n\t" \
               "global_load_lds_dwordx4 %0, %1 offset:2048 sc1\n\tglobal_load_lds_dwordx4 %0, %1 offset:3072 sc1" ::"v"(voff), "s"(src), "s"(dst) : "memory")
#define GLDS1(voff, src, dst) asm volatile("s_mov_b32 m0, %2\n\ts_nop 0\n\tglobal_load_lds_dwordx4 %0, %1 sc1" ::"v"(voff), "s"(src), "s"(dst) : "memory")
#define GLD_A(dst, voff, src, off) asm volatile("global_load_dwordx4 %0, %1, %2 offset:%3 sc1" : "=a"(dst) : "v"(voff), "s"(src), "i"(off) : "memory")
#define GLD_V(dst, voff, src, off) asm volatile("global_load_dwordx4 %0, %1, %2 offset:%3 sc1" : "=v"(dst) : "v"(voff), "s"(src), "i"(off) : "memory")
#define MFMA_AV(acc, a, b) asm volatile("v_mfma_f32_32x32x16_bf16 %0, %1, %2, %0" : "+v"(acc) : "a"(a), "v"(b))
#define MFMA_AV0(acc, a, b) asm volatile("v_mfma_f32_32x32x16_bf16 %0, %1, %2, 0" : "=&v"(acc) : "a"(a), "v"(b))
#define MFMA_VV(acc, a, b) asm volatile("v_mfma_f32_32x32x16_bf16 %0, %1, %2, %0" : "+v"(acc) : "v"(a), "v"(b))
#define MFMA_VV0(acc, a, b) asm volatile("v_mfma_f32_32x32x16_bf16 %0, %1, %2, 0" : "=&v"(acc) : "v"(a), "v"(b))
#define MFMA_AA(acc, a, b) asm volatile("v_mfma_f32_32x32x16_bf16 %0, %1, %2, %0" : "+v"(acc) : "a"(a), "a"(b))
#define MFMA_AA0(acc, a, b) asm volatile("v_mfma_f32_32x32x16_bf16 %0, %1, %2, 0" : "=&v"(acc) : "a"(a), "a"(b))
#define MFMA_VA(acc, a, b) asm volatile("v_mfma_f32_32x32x16_bf16 %0, %1, %2, %0" : "+v"(acc) : "v"(a), "a"(b))
DI u32 cvt_pk_scalar(float a, float b) { u32 r; asm("v_cvt_pk_bf16_f32 %0, %1, %2" : "=v"(r) : "v"(a), "v"(b)); return r; }

static constexpr int NW2A = 16;
struct W2F { u32x4 a[28], b[28]; };
template <int N> DI void w2_load(W2F& f, u32 voff, const char* p0, const char* p1) {
  constexpr int k = N >> 1;
  const char* src = ((N & 1) ? p1 : p0) + (k >> 1) * 4096;
  if constexpr (k < NW2A) { if constexpr (N & 1) GLD_A(f.b[k], voff, src, (k & 1) * 2048); else GLD_A(f.a[k], voff, src, (k & 1) * 2048); }
  else { if constexpr (N & 1) GLD_V(f.b[k], voff, src, (k & 1) * 2048); else GLD_V(f.a[k], voff, src, (k & 1) * 2048); }
}
template <int N0, int N1> DI void w2_range(W2F& f, u32 voff, const char* p0, const char* p1) {
  if constexpr (N0 < N1) { w2_load<N0>(f, voff, p0, p1); w2_range<N0 + 1, N1>(f, voff, p0, p1); }
}
static constexpr int W2_T2 = 20, W2_T3 = 40;

struct G1 {
  u32x4 wf[32];
  float cs[16], bb[16];
  u32 ta[4];
};
template <int M, int J> struct Gap {
  static DI void run(G1& g, W2F& w2, f32x16& acc, const f32x16& prev, u32x2 (&fr)[32][2], float nmu, float rl, float (&gv)[16], float (&ev)[16],
                     u32 (&ov)[8], u32 voff, const char* hsrc, u32 hdst, const char* wp0, const char* wp1) {
    if constexpr (J < 32) {
      constexpr int P = J + 3;
      if constexpr (P < 32) { fr[P][0] = tr_read(g.ta[P >> 3] + M * 32768 + (P & 7) * 1024); fr[P][1] = tr_read(g.ta[P >> 3] + M * 32768 + (P & 7) * 1024 + 256); }
      const u32x4 bf = cat(fr[J][0], fr[J][1]);
      if constexpr (J == 0) MFMA_AV0(acc, g.wf[J], bf); else MFMA_AV(acc, g.wf[J], bf);
      if constexpr (M < 2 && (J & 3) == 1) GLDS1(voff, hsrc + (M + 2) * 32768 + (J >> 2) * 1024, hdst + (M + 2) * 32768 + (J >> 2) * 1024);
      if constexpr (M == 2) w2_range<(J * W2_T2) / 32, ((J + 1) * W2_T2) / 32>(w2, voff, wp0, wp1);
      if constexpr (M == 3) w2_range<W2_T2 + (J * (W2_T3 - W2_T2)) / 32, W2_T2 + ((J + 1) * (W2_T3 - W2_T2)) / 32>(w2, voff, wp0, wp1);
      if constexpr (M > 0) {
        constexpr int V = J >> 1;
        if constexpr ((J & 1) == 0) {
          gv[V] = __builtin_fmaf(__builtin_fmaf(nmu, g.cs[V], prev[V]), rl, g.bb[V]);
          if constexpr (V > 0) { const float p = ev[V - 1] + 1.f; ev[V - 1] = __builtin_amdgcn_rcpf(__builtin_fmaf(p, p, 1.f)); }
        } else {
          ev[V] = __builtin_amdgcn_exp2f(gv[V]);
          if constexpr (V > 0) {
            gv[V - 1] *= __builtin_fmaf(ev[V - 1], -1.3862943611198906f, 0.6931471805599453f);
            if constexpr ((V & 1) == 0) ov[(V - 2) >> 1] = cvt_pk_scalar(gv[V - 2], gv[V - 1]);
          }
        }
      }
      SB();
      Gap<M, J + 1>::run(g, w2, acc, prev, fr, nmu, rl, gv, ev, ov, voff, hsrc, hdst, wp0, wp1);
    }
  }
};
DI void row_stats(u32 sp, int lane, float& nmu, float& rl) {
  const u32 a = sp + (lane & 31) * 8;
  const f32x2 s = (lds_ld<f32x2>(a) + lds_ld<f32x2>(a + 256)) + (lds_ld<f32x2>(a + 512) + lds_ld<f32x2>(a + 768));
  const float mean = s[0] * (1.f / SD);
  const float var = fmaxf(s[1] * (1.f / SD) - mean * mean, 0.f);
  nmu = -mean;
  rl = __builtin_amdgcn_rsqf(var + 1e-5f) * 1.4426950408889634f;
}
template <int M> DI void g1_tile(G1& g, W2F& w2, f32x16& acc, const f32x16& prev, rsrc_t rs, u32 lds0, int w, int lane, u32 voff, u32 act_so,
                                 const char* hsrc, u32 hdst, const char* wp0, const char* wp1) {
  constexpr int NWAIT = M < 2 ? 8 : M == 2 ? 10 : 4 + W2_T2;
  asm volatile("s_waitcnt vmcnt(%0) lgkmcnt(0)\n\ts_barrier" ::"i"(NWAIT) : "memory");
  u32x2 fr[32][2];
#pragma unroll
  for (int p = 0; p < 3; ++p) { fr[p][0] = tr_read(g.ta[0] + M * 32768 + p * 1024); fr[p][1] = tr_read(g.ta[0] + M * 32768 + p * 1024 + 256); }
  float nmu = 0.f, rl = 0.f, gv[16], ev[16];
  u32 ov[8];
  if constexpr (M > 0) row_stats(lds0 + L_SP + (M - 1) * 1024, lane, nmu, rl);
  SB();
  Gap<M, 0>::run(g, w2, acc, prev, fr, nmu, rl, gv, ev, ov, voff, hsrc, hdst, wp0, wp1);
  if constexpr (M > 0) {
    const float p = ev[15] + 1.f;
    const float q = __builtin_amdgcn_rcpf(p * p + 1.f) * -1.3862943611198906f + 0.6931471805599453f;
    ov[7] = pk_bf16(gv[14], gv[15] * q);
    const u32 so = act_so + ((M - 1) >> 1) * (112 * 2048) + ((M - 1) & 1) * 1024;
    st16(rs, voff, so, u32x4{ov[0], ov[1], ov[2], ov[3]});
    st16(rs, voff, so + 2048, u32x4{ov[4], ov[5], ov[6], ov[7]});
  }
  float s1 = 0.f, s2 = 0.f;
#pragma unroll
  for (int j = 0; j < 8; ++j)
#pragma unroll
    for (int e = 0; e < 4; ++e) { const u32 v = fr[j][e >> 1][e & 1]; s1 = dot2bf(v, 0x3f803f80u, s1); s2 = dot2bf(v, v, s2); }
  s1 = half_sum(s1); s2 = half_sum(s2);
  if (lane < 32) lds_st(lds0 + L_SP + M * 1024 + w * 256 + lane * 8, f32x2{s1, s2});
  SB();
}

__global__ __launch_bounds__(256, 1) void s4_mlp(const u16* __restrict__ x, const u16* __restrict__ Are, const u16* __restrict__ Aim,
                                                 const u16* __restrict__ Bre, const u16* __restrict__ Bim, const u16* __restrict__ Cre,
                                                 const u16* __restrict__ Cim, const u16* __restrict__ logdt, const u16* __restrict__ dskip,
                                                 const u16* __restrict__ w1, const u16* __restrict__ b1, const u16* __restrict__ w2,
                                                 u16* __restrict__ out, char* ws) {
  __shared__ __attribute__((aligned(16))) char smem[L_END];
  const u32 lds0 = (u32)(size_t)(LDSP(char))smem;
  const int b = blockIdx.x, tid = threadIdx.x, lane = tid & 63;
  const int w = __builtin_amdgcn_readfirstlane(tid >> 6);
  const int xg = b & 7, bi = b >> 3, r = lane & 31, h = lane >> 5;
  const rsrc_t rs = __builtin_amdgcn_make_buffer_rsrc(ws, 0, WS_BYTES, 0x00020000);
  const u32 voff = lane * 16;
  ST(0, 0);
  const int d0 = 64 * xg + 2 * bi, c = w >> 1, hf = w & 1, d = d0 + c;
  const int pi = d * SN + hf * 64 + lane;
  const u16 r_are = Are[pi], r_aim = Aim[pi], r_bre = Bre[pi], r_bim = Bim[pi], r_cre = Cre[pi], r_cim = Cim[pi];
  const u32x4 xr0 = *(const u32x4*)(x + (8 * b + 2 * w) * SD + lane * 8), xr1 = *(const u32x4*)(x + (8 * b + 2 * w + 1) * SD + lane * 8);
  const u32x2 gen = ld8(rs, 0, OFF_GEN);
  const float ldt = bf2f(logdt[d]);
  const u32 dsk2 = *(const u32*)(dskip + d0);
  {
    u64 pc; asm volatile("s_getpc_b64 %0" : "=s"(pc));
    if (bi < 4) { const u32 t = *(const u32*)((pc & ~127ull) + (u64)(bi * 64 + lane) * 128); asm volatile("" ::"v"(t)); }
  }
  {
    const u32x4 z4 = {0u, 0u, 0u, 0u};
    if (tid < 155) { lds_st(lds0 + L_U + tid * 16, z4); lds_st(lds0 + L_U + 7600 + tid * 16, z4); }
    else if (tid >= 224) lds_st(lds0 + L_R + ((tid - 224) >> 2) * 4160 + 4096 + (tid & 3) * 16, z4);
  }
  u32 ga[12];
#pragma unroll
  for (int j = 0; j < 12; j += 2) {
    const int l0 = 2047 - 8 * tid - j, l1 = l0 - 1;
    const u32 p = pk_bf16((float)l0, (float)l1);
    const int i0 = l0 < 0 ? 648 : l0 < 256 ? l0 : (int)(p & 0xffffu) - (0x4300 - 128);
    const int i1 = l1 < 0 ? 648 : l1 < 256 ? l1 : (int)(p >> 16) - (0x4300 - 128);
    ga[j] = opq(lds0 + L_CMP + 2 * i0);
    ga[j + 1] = opq(lds0 + L_CMP + 2 * i1);
  }
  if (tid < 2) lds_st<u32>(lds0 + L_CMP + tid * 1312 + 1296, 0u);
  const int tri = r < 8 ? r : r < 12 ? 256 + 2 * (r - 8) : r < 16 ? 512 + 256 * ((r - 12) >> 1) + 4 * (r & 1) : r < 21 ? 1024 + 256 * (r - 16) : 0;
  const float trow = (float)tri, tcol = (float)(8 * r);
#pragma unroll
  for (int k = 0; k < 2; ++k) {
    const u32x4 v = k ? xr1 : xr0;
    lds_st(lds0 + L_LN + (2 * w + k) * 1088 + lane * 16, v);
    float e[8];
#pragma unroll
    for (int j = 0; j < 4; ++j) { e[2 * j] = bflo(v[j]); e[2 * j + 1] = bfhi(v[j]); }
    const float mean = wave_sum(((e[0] + e[1]) + (e[2] + e[3])) + ((e[4] + e[5]) + (e[6] + e[7]))) * (1.f / SD);
    float q = 0.f;
#pragma unroll
    for (int j = 0; j < 8; ++j) { const float t = e[j] - mean; q += t * t; }
    const float var = wave_sum(q) * (1.f / SD);
    const float sd = __builtin_amdgcn_sqrtf(rbf(rbf(var) + 1e-5f));
    if (lane == 0) lds_st<u32>(lds0 + L_S1 + (2 * w + k) * 4, pk_bf16(mean, sd));
  }
  bar();
#pragma unroll
  for (int k = 0; k < 2; ++k) {
    const u32 p = lds0 + L_LN + ((lane & 15) >> 2) * 1088 + (128 * w + 64 * k + (lane & 48) + 4 * (lane & 3)) * 2;
    st16(rs, voff, OFF_XT + b * 8192 + w * 2048 + k * 1024, cat(tr_read(p), tr_read(p + 4 * 1088)));
  }
  if (tid < 2) st16(rs, tid * 16, OFF_ST1 + b * 32, lds_ld<u32x4>(lds0 + L_S1 + tid * 16));
  u32x2 tag;
  { const u64 t = (((u64)gen[1] << 32) | gen[0]) + 0x9e3779b97f4a7c15ull;
    tag = u32x2{(u32)__builtin_amdgcn_readfirstlane((u32)t), (u32)__builtin_amdgcn_readfirstlane((u32)(t >> 32))}; }
  ST(0, 1);
  float scale;
  {
    const float dt = rbf(__builtin_amdgcn_exp2f(ldt * 1.4426950408889634f));
    const float a = dt * bf2f(r_are) * 1.4426950408889634f, bq = dt * bf2f(r_aim) * 0.15915494309189535f;
    const float dbr = dt * bf2f(r_bre), dbi = dt * bf2f(r_bim), cre = bf2f(r_cre), cim = bf2f(r_cim);
    const float wr = cre * dbr - cim * dbi, wi = cre * dbi + cim * dbr;
    u32 sb = wave_max(__float_as_uint(fmaxf(fabsf(wr), fabsf(wi)) * __builtin_amdgcn_exp2f(fmaxf(a, 0.f) * 2048.f)));
    sb = min(max((sb + 0x007fffffu) & 0x7f800000u, 0x00800000u), 0x7e800000u);
    scale = __uint_as_float(sb);
    const float inv = __uint_as_float(0x7f000000u - sb);
    lds_st(lds0 + L_TAB + w * 1024 + lane * 16, f32x4{a, bq, wr * inv, wi * inv});
  }
  f32x16 kacc = {};
  auto kstep = [&](int st) {
    u32x4 A, B;
#pragma unroll
    for (int j = 0; j < 4; ++j) {
      const f32x4 P = lds_ld<f32x4>(lds0 + L_TAB + w * 1024 + h * 64 + st * 128 + j * 16);
      const f32x2 ab = {P[0], P[1]};
      const f32x2 e1 = ab * tcol, e2 = ab * trow;
      const float m1 = __builtin_amdgcn_exp2f(e1[0]), p1 = __builtin_amdgcn_fractf(e1[1]);
      const float m2 = __builtin_amdgcn_exp2f(e2[0]), p2 = __builtin_amdgcn_fractf(e2[1]);
      const f32x2 f = f32x2{__builtin_amdgcn_cosf(p1), -__builtin_amdgcn_sinf(p1)} * m1;
      const f32x2 g = f32x2{__builtin_amdgcn_cosf(p2), __builtin_amdgcn_sinf(p2)} * m2;
      const f32x2 e = f32x2{P[2], P[3]} * g[0] + f32x2{-P[3], P[2]} * g[1];
      B[j] = pk_f16(f[0], f[1]);
      A[j] = pk_f16(e[0], e[1]);
    }
    kacc = mfma_f16(A, B, kacc);
  };
#pragma unroll 1
  for (int st = 0; st < 2; ++st) kstep(st);
  bar_vm();
  flag_all(rs, OFF_FA, b, tid, tag);
  const int fp = tid & 31, o = tid >> 5;
  const int ftp = b % 28, dq = b / 28;
  u32 rw1[8], rw2[8];
  if (b < 224) {
    const u32* s1 = (const u32*)(w1 + (size_t)(64 * dq + 8 * o) * SF + 64 * ftp + 2 * fp);
    const u32* s2 = (const u32*)(w2 + (size_t)(16 * (4 * bi + (o >> 1)) + 4 * (o & 1)) * SD + 64 * xg + 2 * fp);
#pragma unroll
    for (int j = 0; j < 8; ++j) rw1[j] = s1[j * (SF / 2)];
#pragma unroll
    for (int j = 0; j < 8; ++j) rw2[j] = s2[(8 * (j >> 2) + (j & 3)) * (SD / 2)];
  }
#pragma unroll 1
  for (int st = 2; st < 8; ++st) kstep(st);
  ST(0, 2);
  Poll pl;
  if (w == 0) pl = poll_issue(rs, OFF_FA, b, lane);
#pragma unroll
  for (int g = 0; g < 3; ++g)
    lds_st(lds0 + L_PART + w * 3584 + r * 112 + g * 32 + h * 16, f32x4{kacc[4 * g], kacc[4 * g + 1], kacc[4 * g + 2], kacc[4 * g + 3]} * scale);
  if (b < 224) {
    u32x4 f0, f1;
    float s0 = 0.f, s1 = 0.f;
#pragma unroll
    for (int j = 0; j < 4; ++j) {
      f0[j] = __builtin_amdgcn_perm(rw1[2 * j + 1], rw1[2 * j], 0x05040100u);
      f1[j] = __builtin_amdgcn_perm(rw1[2 * j + 1], rw1[2 * j], 0x07060302u);
      s0 = dot2bf(f0[j], 0x3f803f80u, s0);
      s1 = dot2bf(f1[j], 0x3f803f80u, s1);
    }
    const u32 v1 = (((fp >> 4) * 32 + (o >> 1)) * 64 + (o & 1) * 32 + 2 * (fp & 15)) * 16;
    st16(rs, v1, OFF_W1T + (2 * ftp * 32 + 4 * dq) * 1024, f0);
    st16(rs, v1 + 16, OFF_W1T + (2 * ftp * 32 + 4 * dq) * 1024, f1);
    s0 = half_sum(s0); s1 = half_sum(s1);
    if (lane < 32) lds_st(lds0 + L_CS + w * 256 + lane * 8, f32x2{s0, s1});
#pragma unroll
    for (int j = 0; j < 4; ++j) {
      f0[j] = __builtin_amdgcn_perm(rw2[2 * j + 1], rw2[2 * j], 0x05040100u);
      f1[j] = __builtin_amdgcn_perm(rw2[2 * j + 1], rw2[2 * j], 0x07060302u);
    }
    const u32 v2 = (((o >> 1) * 2 + (fp >> 4)) * 64 + (o & 1) * 32 + 2 * (fp & 15)) * 16;
    st16(rs, v2, OFF_W2T + (xg * 112 + 4 * bi) * 2048, f0);
    st16(rs, v2 + 16, OFF_W2T + (xg * 112 + 4 * bi) * 2048, f1);
  }
  bar();
  if (tid < 192) {
    const int col = tid & 31, q = tid >> 5;
#pragma unroll
    for (int cc = 0; cc < 2; ++cc) {
      const u32 pp = lds0 + L_PART + cc * 7168 + col * 112 + q * 16;
      const f32x4 s = lds_ld<f32x4>(pp) + lds_ld<f32x4>(pp + 3584);
      const u32 p0 = pk_bf16(s[0], s[1]), p1 = pk_bf16(s[2], s[3]);
      const u32 cm = lds0 + L_CMP + cc * 1312;
      if (q < 2) lds_st(cm + 16 * col + 8 * q, u32x2{p0, p1});
      else if (q == 2) lds_st(cm + 512 + 8 * col, u32x2{p0, p1});
      else if (q == 3) { lds_st<u32>(cm + 768 + 4 * col, p0); lds_st<u32>(cm + 896 + 4 * col, p1); }
      else if (q == 4) { lds_st<u16>(cm + 1024 + 2 * col, p0); lds_st<u16>(cm + 1088 + 2 * col, p0 >> 16); lds_st<u16>(cm + 1152 + 2 * col, p1); lds_st<u16>(cm + 1216 + 2 * col, p1 >> 16); }
      else if (col == 0) lds_st<u16>(cm + 1280, p0);
    }
  } else if (b < 224) {
    const u32 a = lds0 + L_CS + lane * 4;
    st4(rs, lane * 4, OFF_CSP + (dq * SF + 64 * ftp) * 4, __float_as_uint((lds_ld<float>(a) + lds_ld<float>(a + 256)) + (lds_ld<float>(a + 512) + lds_ld<float>(a + 768))));
  }
  if (w == 0) poll_wait(pl, rs, OFF_FA, b, lane, tag);
  bar();
  ST(0, 3);
  const u32x4 xa = ld16(rs, tid * 8192, OFF_XT + d0 * 16), xb = ld16(rs, tid * 8192 + 16, OFF_XT + d0 * 16);
  const u32x4 s0 = ld16(rs, tid * 32, OFF_ST1), s1 = ld16(rs, tid * 32 + 16, OFF_ST1);
#pragma unroll
  for (int cc = 0; cc < 2; ++cc) {
    u32 k[6], od[5];
#pragma unroll
    for (int j = 0; j < 6; ++j) k[j] = (u32)lds_ld<u16>(ga[2 * j] + cc * 1312) | ((u32)lds_ld<u16>(ga[2 * j + 1] + cc * 1312) << 16);
#pragma unroll
    for (int j = 0; j < 5; ++j) od[j] = __builtin_amdgcn_alignbit(k[j + 1], k[j], 16);
    const u32 rp = lds0 + L_R + cc * 16640 + tid * 16;
    lds_st(rp, u32x4{k[0], k[1], k[2], k[3]});
    lds_st(rp + 4160, u32x4{od[0], od[1], od[2], od[3]});
    lds_st(rp + 8320, u32x4{k[1], k[2], k[3], k[4]});
    lds_st(rp + 12480, u32x4{od[1], od[2], od[3], od[4]});
  }
  u32x4 ua, ub;
  {
    float mu[8], ri[8];
#pragma unroll
    for (int j = 0; j < 4; ++j) {
      mu[j] = bflo(s0[j]); ri[j] = __builtin_amdgcn_rcpf(bfhi(s0[j]));
      mu[4 + j] = bflo(s1[j]); ri[4 + j] = __builtin_amdgcn_rcpf(bfhi(s1[j]));
    }
#pragma unroll
    for (int j = 0; j < 4; ++j) {
      ua[j] = pk_bf16((bflo(xa[j]) - mu[2 * j]) * ri[2 * j], (bfhi(xa[j]) - mu[2 * j + 1]) * ri[2 * j + 1]);
      ub[j] = pk_bf16((bflo(xb[j]) - mu[2 * j]) * ri[2 * j], (bfhi(xb[j]) - mu[2 * j + 1]) * ri[2 * j + 1]);
    }
    const u32 up = lds0 + L_U + (31 + (tid >> 2)) * 80 + (tid & 3) * 16;
    lds_st(up, ua);
    lds_st(up + 7600, ub);
  }
  bar_vm();
  flag_all(rs, OFF_FB, b, tid, tag);
  ST(0, 4);
  f32x16 acc0, acc1;
  float base[16];
  {
    const int cc = (3 - r) & 3;
    const u32 gA = opq(lds0 + L_R + c * 16640 + cc * 4160 + 2 * (2047 - r + 8 * h - cc) - 32 * (32 * hf + 30) - 2048), gB = opq(gA + 8);
    const u32 up = opq(lds0 + L_U + c * 7600 + (16 + r - 16 * hf) * 80 + 16 * h);
    u32x2 l0[32], l1[32], h0[32], h1[32];
    u32x4 b0[32], b1v[32];
#define OG(n) (32 * (30 - 2 * ((n) >> 1) + ((n) & 1)))
#define OU(n) (80 * (15 - ((n) >> 1)) + 32 * ((n) & 1))
#pragma unroll
    for (int n = 0; n < 2; ++n) {
      l0[n] = lds_ld<u32x2>(gA + 2048 + OG(n)); l1[n] = lds_ld<u32x2>(gB + 2048 + OG(n)); SB();
      b1v[n] = lds_ld<u32x4>(up + OU(n) + 2560); b0[n] = lds_ld<u32x4>(up + OU(n)); SB();
      h0[n] = lds_ld<u32x2>(gA + OG(n)); h1[n] = lds_ld<u32x2>(gB + OG(n)); SB();
    }
#pragma unroll
    for (int n = 0; n < 32; ++n) {
      const int m = n + 2;
      const u32x4 glo = cat(l0[n], l1[n]), ghi = cat(h0[n], h1[n]);
      if (n == 0) MFMA_VV0(acc1, glo, b1v[n]); else MFMA_VV(acc1, glo, b1v[n]);
      if (m < 32) { l0[m] = lds_ld<u32x2>(gA + 2048 + OG(m)); l1[m] = lds_ld<u32x2>(gB + 2048 + OG(m)); }
      SB();
      if (n == 0) MFMA_VV0(acc0, glo, b0[n]); else MFMA_VV(acc0, glo, b0[n]);
      if (m < 32) { b1v[m] = lds_ld<u32x4>(up + OU(m) + 2560); b0[m] = lds_ld<u32x4>(up + OU(m)); }
      if (n >= 1 && n < 17) {
        const int e = n - 1, j = (e >> 1) & 3;
        const u32 xv = e < 8 ? xa[j] : xb[j], uv = e < 8 ? ua[j] : ub[j];
        base[e] = __builtin_fmaf(e < 8 ? bflo(dsk2) : bfhi(dsk2), (e & 1) ? bfhi(uv) : bflo(uv), (e & 1) ? bfhi(xv) : bflo(xv));
        asm volatile("" : "+v"(base[e]));
      }
      SB();
      MFMA_VV(acc1, ghi, b0[n]);
      if (m < 32) { h0[m] = lds_ld<u32x2>(gA + OG(m)); h1[m] = lds_ld<u32x2>(gB + OG(m)); }
      if (n == 24 && w == 0) pl = poll_issue(rs, OFF_FB, b, lane);
      SB();
    }
    asm volatile("s_nop 11" : "+v"(acc0), "+v"(acc1));
  }
  ST(0, 5);
  {
    const u32 rp = lds0 + L_RED + w * 9216 + r * 144 + h * 16;
#pragma unroll
    for (int g = 0; g < 4; ++g) lds_st(rp + g * 32, f32x4{acc0[4 * g], acc0[4 * g + 1], acc0[4 * g + 2], acc0[4 * g + 3]});
#pragma unroll
    for (int g = 0; g < 4; ++g) lds_st(rp + 4608 + g * 32, f32x4{acc1[4 * g], acc1[4 * g + 1], acc1[4 * g + 2], acc1[4 * g + 3]});
    if (w == 0) poll_wait(pl, rs, OFF_FB, b, lane, tag);
    bar();
    const u32 pp = lds0 + L_RED + (tid >> 2) * 144 + (tid & 3) * 32;
#pragma unroll
    for (int cc = 0; cc < 2; ++cc) {
      const f32x4 t0 = lds_ld<f32x4>(pp + cc * 18432) + lds_ld<f32x4>(pp + cc * 18432 + 9216);
      const f32x4 t1 = lds_ld<f32x4>(pp + cc * 18432 + 16) + lds_ld<f32x4>(pp + cc * 18432 + 9216 + 16);
      const float* bs = base + 8 * cc;
      st16(rs, (tid >> 2) * 32768 + (tid & 3) * 16, OFF_HT + (d0 + cc) * 64,
           u32x4{pk_bf16(bs[0] + t0[0], bs[1] + t0[1]), pk_bf16(bs[2] + t0[2], bs[3] + t0[3]), pk_bf16(bs[4] + t1[0], bs[5] + t1[1]), pk_bf16(bs[6] + t1[2], bs[7] + t1[3])});
    }
  }
  bar_vm();
  flag_all(rs, OFF_FC, b, tid, tag);
  ST(0, 6);
  const int ltp = 4 * xg + (bi >> 3), dtp = bi & 7;
  const char* wp = ws + OFF_W2T + (size_t)(dtp * 112 + 28 * w) * 2048;
  const char* wp0 = wp + (w >> 1) * 1024;
  const char* wp1 = wp + ((w >> 1) ^ 1) * 1024;
  const char* hres = ws + OFF_HT + (size_t)((2 * ltp + (w & 1)) * SD + 64 * dtp + 32 * (w >> 1)) * 64;
  const u32 hres_dst = lds0 + L_HR + w * 2048;
  W2F w2f;
  if (bi < 28) {
    const int p = 2 * xg + (bi >= 14), g14 = bi >= 14 ? bi - 14 : bi, ft = 4 * g14 + w;
    G1 g;
#pragma unroll
    for (int q = 0; q < 4; ++q) {
      const char* src = ws + OFF_W1T + (size_t)(ft * 32 + ((w + q) & 3) * 8) * 1024 + 4096;
#pragma unroll
      for (int j = 0; j < 8; ++j) GLD_A(g.wf[8 * q + j], voff, src, j * 1024 - 4096);
    }
    if (tid < 128) {
      const int f = 128 * g14 + tid;
      float s = 0.f;
#pragma unroll
      for (int j = 0; j < 8; ++j) s += __uint_as_float(ld4(rs, f * 4, OFF_CSP + j * SF * 4));
      lds_st(lds0 + L_CB + tid * 8, f32x2{s, bf2f(b1[f]) * 1.4426950408889634f});
    }
    if (w == 0) poll_wait(poll_issue(rs, OFF_FC, b, lane), rs, OFF_FC, b, lane, tag);
    bar_vm();
    ST(0, 7);
    if (b == 0 && tid == 0) st8(rs, 0, OFF_GEN, tag);
    const char* hsrc = ws + OFF_HT + (size_t)p * 131072 + w * 8192;
    const u32 hdst = lds0 + L_H + w * 8192;
    GLDS1(voff, hres, hres_dst); GLDS1(voff, hres + 1024, hres_dst + 1024);
    GLDS4(voff, hsrc, hdst); GLDS4(voff, hsrc + 4096, hdst + 4096);
    GLDS4(voff, hsrc + 32768, hdst + 32768); GLDS4(voff, hsrc + 32768 + 4096, hdst + 32768 + 4096);
#pragma unroll
    for (int q = 0; q < 8; ++q) {
      const f32x4 t = lds_ld<f32x4>(lds0 + L_CB + (32 * w + 8 * (q >> 1) + 4 * h + 2 * (q & 1)) * 8);
      g.cs[2 * q] = t[0]; g.bb[2 * q] = t[1]; g.cs[2 * q + 1] = t[2]; g.bb[2 * q + 1] = t[3];
    }
#pragma unroll
    for (int q = 0; q < 4; ++q) g.ta[q] = opq(lds0 + L_H + ((w + q) & 3) * 8192 + (8 * h + ((lane & 15) >> 2)) * 64 + (lane & 16) * 2 + (lane & 3) * 8);
    const u32 act_so = OFF_ACT + (2 * p * 112 + 2 * ft) * 2048;
    f32x16 ac0, ac1;
    g1_tile<0>(g, w2f, ac0, ac0, rs, lds0, w, lane, voff, act_so, hsrc, hdst, wp0, wp1);
    ST(1, 0);
    g1_tile<1>(g, w2f, ac1, ac0, rs, lds0, w, lane, voff, act_so, hsrc, hdst, wp0, wp1);
    ST(1, 1);
    g1_tile<2>(g, w2f, ac0, ac1, rs, lds0, w, lane, voff, act_so, hsrc, hdst, wp0, wp1);
    ST(1, 2);
    g1_tile<3>(g, w2f, ac1, ac0, rs, lds0, w, lane, voff, act_so, hsrc, hdst, wp0, wp1);
    ST(1, 3);
    bar();
    {
      float nmu, rl;
      row_stats(lds0 + L_SP + 3 * 1024, lane, nmu, rl);
      u32 ov[8];
#pragma unroll
      for (int j = 0; j < 8; ++j) {
        const f32x2 t = f32x2{g.cs[2 * j], g.cs[2 * j + 1]} * nmu + f32x2{ac1[2 * j], ac1[2 * j + 1]};
        const f32x2 gv = t * rl + f32x2{g.bb[2 * j], g.bb[2 * j + 1]};
        const f32x2 pv = f32x2{__builtin_amdgcn_exp2f(gv[0]), __builtin_amdgcn_exp2f(gv[1])} + 1.f;
        const f32x2 dv = pv * pv + 1.f;
        const f32x2 rv = gv * (f32x2{__builtin_amdgcn_rcpf(dv[0]), __builtin_amdgcn_rcpf(dv[1])} * -1.3862943611198906f + 0.6931471805599453f);
        ov[j] = pk_bf16(rv[0], rv[1]);
      }
      st16(rs, voff, act_so + 112 * 2048 + 1024, u32x4{ov[0], ov[1], ov[2], ov[3]});
      st16(rs, voff, act_so + 112 * 2048 + 1024 + 2048, u32x4{ov[4], ov[5], ov[6], ov[7]});
    }
    bar_vm();
    if (tid == 0) st8(rs, 0, OFF_FD + p * 256 + g14 * 16, tag);
    ST(1, 4);
  } else {
    w2_range<0, W2_T3>(w2f, voff, wp0, wp1);
    if (w == 0) poll_wait(poll_issue(rs, OFF_FC, b, lane), rs, OFF_FC, b, lane, tag);
    bar_vm();
    GLDS1(voff, hres, hres_dst); GLDS1(voff, hres + 1024, hres_dst + 1024);
  }
  w2_range<W2_T3, 56>(w2f, voff, wp0, wp1);
  if (w == 0) {
#pragma unroll 1
    for (int sp = 0; sp < (1 << 16); ++sp) {
      const u32x2 v = ld8(rs, min(lane, 13) * 16, OFF_FD + (ltp >> 1) * 256);
      if (__all(v[0] == tag[0] && v[1] == tag[1])) break;
      __builtin_amdgcn_s_sleep(1);
      asm volatile("" ::: "memory");
    }
  }
  bar_vm();
  ST(1, 5);
  {
    const char* ap = ws + OFF_ACT + (size_t)(ltp * 112 + 28 * w) * 2048;
    const char* ap0 = ap + (w & 1) * 1024;
    const char* ap1 = ap + ((w & 1) ^ 1) * 1024;
    u32x4 aa[28], ab[28];
    constexpr int DEP = 16;
#pragma unroll
    for (int k = 0; k < DEP; ++k) { GLD_A(aa[k], voff, ap0 + (k >> 1) * 4096, (k & 1) * 2048); GLD_A(ab[k], voff, ap1 + (k >> 1) * 4096, (k & 1) * 2048); }
    f32x16 acc[4];
#pragma unroll
    for (int k = 0; k < 28; ++k) {
      const int left = (k + DEP < 28 ? k + DEP : 28) - k - 1;
      asm volatile("s_waitcnt vmcnt(%2)" : "+a"(aa[k]), "+a"(ab[k]) : "i"(2 * left) : "memory");
      if (k == 0) { MFMA_AA0(acc[0], w2f.a[k], aa[k]); MFMA_AA0(acc[1], w2f.a[k], ab[k]); MFMA_AA0(acc[2], w2f.b[k], aa[k]); MFMA_AA0(acc[3], w2f.b[k], ab[k]); }
      else if (k < NW2A) { MFMA_AA(acc[0], w2f.a[k], aa[k]); MFMA_AA(acc[1], w2f.a[k], ab[k]); MFMA_AA(acc[2], w2f.b[k], aa[k]); MFMA_AA(acc[3], w2f.b[k], ab[k]); }
      else { MFMA_VA(acc[0], w2f.a[k], aa[k]); MFMA_VA(acc[1], w2f.a[k], ab[k]); MFMA_VA(acc[2], w2f.b[k], aa[k]); MFMA_VA(acc[3], w2f.b[k], ab[k]); }
      if (k + DEP < 28) { const int n = k + DEP; GLD_A(aa[n], voff, ap0 + (n >> 1) * 4096, (n & 1) * 2048); GLD_A(ab[n], voff, ap1 + (n >> 1) * 4096, (n & 1) * 2048); }
    }
    asm volatile("s_nop 15" : "+v"(acc[0]), "+v"(acc[1]), "+v"(acc[2]), "+v"(acc[3]));
    ST(1, 6);
#pragma unroll
    for (int s = 1; s < 4; ++s) {
      const u32 rp = lds0 + ((w ^ s) * 3 + s - 1) * 4096 + lane * 16;
#pragma unroll
      for (int g = 0; g < 4; ++g) lds_st(rp + g * 1024, f32x4{acc[s][4 * g], acc[s][4 * g + 1], acc[s][4 * g + 2], acc[s][4 * g + 3]});
    }
    const u32 hp = hres_dst + (8 * h + ((lane & 15) >> 2)) * 64 + (lane & 16) * 2 + (lane & 3) * 8;
    const u32x4 hr0 = cat(tr_read(hp), tr_read(hp + 256)), hr1 = cat(tr_read(hp + 1024), tr_read(hp + 1280));
    bar();
    f32x4 v[4];
#pragma unroll
    for (int g = 0; g < 4; ++g) {
      const u32 rp = lds0 + w * 3 * 4096 + g * 1024 + lane * 16;
      v[g] = (f32x4{acc[0][4 * g], acc[0][4 * g + 1], acc[0][4 * g + 2], acc[0][4 * g + 3]} + lds_ld<f32x4>(rp)) + (lds_ld<f32x4>(rp + 4096) + lds_ld<f32x4>(rp + 8192));
    }
    u32x4* op = (u32x4*)(out + (size_t)(64 * ltp + 32 * (w & 1) + r) * SD + 64 * dtp + 32 * (w >> 1) + 8 * h);
#pragma unroll
    for (int k = 0; k < 4; k += 2) {
      const u32x4 hr = k ? hr1 : hr0;
      float lo[4], hi[4];
#pragma unroll
      for (int e = 0; e < 4; ++e) {
        auto q = __builtin_amdgcn_permlane32_swap(__float_as_uint(v[k][e]), __float_as_uint(v[k + 1][e]), false, false);
        lo[e] = __uint_as_float(q[0]); hi[e] = __uint_as_float(q[1]);
      }
      op[k] = u32x4{pk_bf16(lo[0] + bflo(hr[0]), lo[1] + bfhi(hr[0])), pk_bf16(lo[2] + bflo(hr[1]), lo[3] + bfhi(hr[1])),
                    pk_bf16(hi[0] + bflo(hr[2]), hi[1] + bfhi(hr[2])), pk_bf16(hi[2] + bflo(hr[3]), hi[3] + bfhi(hr[3]))};
    }
  }
  ST(1, 7);
}

extern "C" void kernel_launch(void* const* d_in, const int* in_sizes, int n_in, void* d_out, int out_size, void* d_ws, size_t ws_size,
                              hipStream_t stream) {
  s4_mlp<<<256, 256, 0, stream>>>((const u16*)d_in[0], (const u16*)d_in[1], (const u16*)d_in[2], (const u16*)d_in[3], (const u16*)d_in[4],
                                  (const u16*)d_in[5], (const u16*)d_in[6], (const u16*)d_in[7], (const u16*)d_in[8], (const u16*)d_in[10],
                                  (const u16*)d_in[11], (const u16*)d_in[12], (u16*)d_out, (char*)d_ws);
}
```

```cpp
#include <hip/hip_runtime.h>
#include <stdint.h>

typedef unsigned short u16;
typedef unsigned int u32;
typedef unsigned long long u64;
using u32x2 = __attribute__((ext_vector_type(2))) unsigned;
using u32x4 = __attribute__((ext_vector_type(4))) unsigned;
using f32x2 = __attribute__((ext_vector_type(2))) float;
using f32x4 = __attribute__((ext_vector_type(4))) float;
using f32x16 = __attribute__((ext_vector_type(16))) float;
using s16x4 = __attribute__((ext_vector_type(4))) short;
using bf16x2 = __attribute__((ext_vector_type(2))) __bf16;
using h16x2 = __attribute__((ext_vector_type(2))) _Float16;
typedef __amdgpu_buffer_rsrc_t rsrc_t;
#define DI __device__ __forceinline__
#define LDSP(T) __attribute__((address_space(3))) T*
#define SB() __builtin_amdgcn_sched_barrier(0)

static constexpr int SL = 2048, SD = 512, SN = 128, SF = 1792;
static constexpr float LOG2E = 1.4426950408889634f, LN2 = 0.6931471805599453f;

static constexpr u32 OFF_XT = 0, XT_S = 16640;
static constexpr u32 OFF_HT = 4u << 20, HT_S = 33024;
static constexpr u32 OFF_W1T = 8u << 20;
static constexpr u32 OFF_W2T = 10u << 20;
static constexpr u32 OFF_ACT = 12u << 20;
static constexpr u32 OFF_ST1 = 20u << 20;
static constexpr u32 OFF_CSP = OFF_ST1 + 65536;
static constexpr u32 OFF_FA = 21u << 20;
static constexpr u32 OFF_FB = OFF_FA + 32768;
static constexpr u32 OFF_FC1 = OFF_FB + 32768;
static constexpr u32 OFF_FC2 = OFF_FC1 + 32768;
static constexpr u32 OFF_FD = OFF_FC2 + 32768;
static constexpr u32 FD_S = 128, FD_T = 16 * FD_S;
static constexpr u32 OFF_GEN = OFF_FD + 64 * FD_T;
static constexpr u32 WS_BYTES = 22u << 20;
static constexpr u32 OOB = 0x7ffffff0u, FAR = 0x40000000u;

static constexpr int L_R = 0;
static constexpr int L_U = 33280;
static constexpr int L_CMP = 48640;
static constexpr int L_PART = 52352;
static constexpr int L_EF = 66688;
static constexpr int L_WS = 124416;
static constexpr int L_LN = 145408;
static constexpr int L_CS = 150528;
static constexpr int L_RED = 131072;
static constexpr int L_HL = 0;
static constexpr int L_HE = 65536;
static constexpr int L_HR = 135168;
static constexpr int L_X = 144384;
static constexpr int L_CB = 160768;
static constexpr int L_SP = 161792;
static constexpr int L_G2 = 0;
static constexpr int L_END = 163840;

struct GatherTab { u32 v[3][256][4]; };
static constexpr GatherTab make_gather() {
  GatherTab g = {};
  for (int t = 0; t < 256; ++t)
    for (int j = 0; j < 12; ++j) {
      const int lam = 2047 - 8 * t - j;
      u32 off = 32 * 112;
      if (lam >= 0) {
        int sh = 0;
        while ((lam >> sh) >= 256) ++sh;
        int q = lam >> sh;
        if (sh > 0) { const int rem = lam & ((1 << sh) - 1), half = 1 << (sh - 1); if (rem > half || (rem == half && (q & 1))) ++q; }
        const int tt = q << sh, col = (tt >> 3) & 31, hi = tt >> 8, bb = tt & 7;
        const int row = hi == 0 ? bb : hi == 1 ? 8 + bb / 2 : hi < 4 ? 12 + 2 * (hi - 2) + bb / 4 : 12 + hi;
        off = col * 112 + row * 4;
      }
      g.v[j >> 2][t][j & 3] = off;
    }
  return g;
}
static __device__ const GatherTab gather_tab = make_gather();

DI float bflo(u32 w) { return __uint_as_float(w << 16); }
DI float bfhi(u32 w) { return __uint_as_float(w & 0xffff0000u); }
DI float bf2f(u16 x) { return __uint_as_float((u32)x << 16); }
DI u32 pk_bf16(float a, float b) { f32x2 v = {a, b}; return __builtin_bit_cast(u32, __builtin_convertvector(v, bf16x2)); }
DI u32 pk_f16(float a, float b) { f32x2 v = {a, b}; return __builtin_bit_cast(u32, __builtin_convertvector(v, h16x2)); }
DI float rbf(float a) { return bflo(pk_bf16(a, a)); }
DI float dot2bf(u32 a, u32 b, float c) { return __builtin_amdgcn_fdot2_f32_bf16(__builtin_bit_cast(bf16x2, a), __builtin_bit_cast(bf16x2, b), c, false); }
template <int CTRL> DI u32 dpp(u32 v) { return __builtin_amdgcn_update_dpp(0u, v, CTRL, 0xf, 0xf, true); }
template <int CTRL> DI float dppf(float v) { return __uint_as_float(dpp<CTRL>(__float_as_uint(v))); }
DI float fold32(float a, float b) {
  auto q = __builtin_amdgcn_permlane32_swap(__float_as_uint(a), __float_as_uint(b), false, false);
  return __uint_as_float(q[0]) + __uint_as_float(q[1]);
}
DI float sum32(float v) {
  v += dppf<0x128>(v); v += dppf<0x124>(v); v += dppf<0x122>(v); v += dppf<0x121>(v);
  auto r = __builtin_amdgcn_permlane16_swap(__float_as_uint(v), __float_as_uint(v), false, false);
  return __uint_as_float(r[0]) + __uint_as_float(r[1]);
}
DI u32 wave_max(u32 v) {
  v = max(v, dpp<0x128>(v)); v = max(v, dpp<0x124>(v)); v = max(v, dpp<0x122>(v)); v = max(v, dpp<0x121>(v));
  auto r = __builtin_amdgcn_permlane16_swap(v, v, false, false);
  v = max(r[0], r[1]);
  auto q = __builtin_amdgcn_permlane32_swap(v, v, false, false);
  return max(q[0], q[1]);
}
struct Cm { f32x2 a, s; };
DI Cm cm(f32x2 a) { return Cm{a, f32x2{-a[1], a[0]}}; }
DI f32x2 cexp_rev(float e, float p) {
  p = __builtin_amdgcn_fractf(p);
  return f32x2{__builtin_amdgcn_cosf(p), __builtin_amdgcn_sinf(p)} * __builtin_amdgcn_exp2f(e);
}
DI u32 opq(u32 v) { asm volatile("" : "+v"(v)); return v; }
DI float opqf(float v) { asm volatile("" : "+v"(v)); return v; }
template <class T> DI T lds_ld(u32 a) { return *(LDSP(const T))(size_t)a; }
template <class T> DI void lds_st(u32 a, T v) { *(LDSP(T))(size_t)a = v; }
DI u32x2 tr_read(u32 a) { return __builtin_bit_cast(u32x2, __builtin_amdgcn_ds_read_tr16_b64_v4i16((LDSP(s16x4))(size_t)a)); }
DI u32x4 cat(u32x2 a, u32x2 b) { return u32x4{a[0], a[1], b[0], b[1]}; }
DI void bar() { asm volatile("s_waitcnt lgkmcnt(0)\n\ts_barrier" ::: "memory"); }

DI void st16(rsrc_t r, u32 vo, u32 so, u32x4 v) { __builtin_amdgcn_raw_buffer_store_b128(v, r, opq(vo + so), 0, 16); }
DI void st4(rsrc_t r, u32 vo, u32 so, u32 v) { __builtin_amdgcn_raw_buffer_store_b32(v, r, vo, so, 16); }
DI u32x4 ld16(rsrc_t r, u32 vo, u32 so) { return __builtin_amdgcn_raw_buffer_load_b128(r, vo, so, 16); }
DI void flag_st(rsrc_t r, u32 vo, u32 so, u64 tag) { __builtin_amdgcn_raw_buffer_store_b64(u32x2{(u32)tag, (u32)(tag >> 32)}, r, vo, so, 16); }
DI u64 poll_issue(rsrc_t rs, u32 so, u32 vo) {
  u64 v;
  asm volatile("buffer_load_dwordx2 %0, %1, %2, %3 offen sc1" : "=v"(v) : "v"(vo), "s"(rs), "s"(so) : "memory");
  return v;
}
template <int N> DI void poll_wait(u64 v, rsrc_t rs, u32 so, u32 vo, u64 tag) {
  u32 cnt = 1u << 16;
  asm volatile("s_waitcnt vmcnt(%6)\n"
               "1:\n\tv_cmp_ne_u64 vcc, %5, %0\n\ts_cmp_eq_u64 vcc, 0\n\ts_cbranch_scc1 2f\n\t"
               "s_sub_u32 %1, %1, 1\n\ts_cbranch_scc1 2f\n\ts_sleep 1\n\t"
               "buffer_load_dwordx2 %0, %2, %3, %4 offen sc1\n\ts_waitcnt vmcnt(0)\n\ts_branch 1b\n2:"
               : "+v"(v), "+s"(cnt) : "v"(vo), "s"(rs), "s"(so), "s"(tag), "i"(N) : "vcc", "scc", "memory");
}
#define GLDS1(voff, src, dst) asm volatile("s_mov_b32 m0, %2\n\ts_nop 0\n\tglobal_load_lds_dwordx4 %0, %1" ::"v"(voff), "s"(src), "s"(dst) : "memory")
#define BLDS1(voff, so, dst) asm volatile("s_mov_b32 m0, %3\n\ts_nop 0\n\tbuffer_load_dwordx4 %0, %1, %2 offen sc1 lds" ::"v"(voff), "s"(rs), "s"(so), "s"(dst) : "memory")
#define BLDS1N(voff, so, off) asm volatile("buffer_load_dwordx4 %0, %1, %2 offen offset:%3 sc1 lds" ::"v"(voff), "s"(rs), "s"(so), "i"(off) : "memory")
#define BLDS4(voff, so, dst)                                                                                                      \
  asm volatile("s_mov_b32 m0, %3\n\ts_nop 0\n\tbuffer_load_dwordx4 %0, %1, %2 offen sc1 lds\n\tbuffer_load_dwordx4 %0, %1, %2 offen offset:1024 sc1 lds\n\t" \
               "buffer_load_dwordx4 %0, %1, %2 offen offset:2048 sc1 lds\n\tbuffer_load_dwordx4 %0, %1, %2 offen offset:3072 sc1 lds" \
               ::"v"(voff), "s"(rs), "s"(so), "s"(dst) : "memory")
#define BLD_A(dst, voff, so, off) asm volatile("buffer_load_dwordx4 %0, %1, %2, %3 offen offset:%4 sc1" : "=a"(dst) : "v"(voff), "s"(rs), "s"(so), "i"(off) : "memory")
#define BLD_V(dst, voff, so, off) asm volatile("buffer_load_dwordx4 %0, %1, %2, %3 offen offset:%4 sc1" : "=v"(dst) : "v"(voff), "s"(rs), "s"(so), "i"(off) : "memory")
#define MF32 "v_mfma_f32_32x32x16_bf16 %0, %1, %2, "
#define MFMA_AV(acc, a, b) asm volatile(MF32 "%0" : "+v"(acc) : "a"(a), "v"(b))
#define MFMA_AV2(acc, a, b, n) asm volatile(MF32 "%0" : "+v"(acc) : "a"(a), "v"(b), "v"(n))
#define MFMA_AV0(acc, a, b) asm volatile(MF32 "0" : "=&v"(acc) : "a"(a), "v"(b))
#define MFMA_VV(acc, a, b) asm volatile(MF32 "%0" : "+v"(acc) : "v"(a), "v"(b))
#define MFMA_VV0(acc, a, b) asm volatile(MF32 "0" : "=&v"(acc) : "v"(a), "v"(b))
#define MFMA_VA(acc, a, b) asm volatile(MF32 "%0" : "+v"(acc) : "v"(a), "a"(b))
#define MFMA_H(acc, a, b) asm volatile("v_mfma_f32_32x32x16_f16 %0, %1, %2, %0" : "+v"(acc) : "v"(a), "v"(b))
#define MFMA_H0(acc, a, b) asm volatile("v_mfma_f32_32x32x16_f16 %0, %1, %2, 0" : "=&v"(acc) : "v"(a), "v"(b))

static constexpr int W2T_AT = 1, FB_POLL = 24;
static constexpr int FC1_SET = 10;
static constexpr int W2_N1 = 6, W2_N0 = 10;
static constexpr int W2_N2 = 4, W2_N2B = 6;
static constexpr int W2_N4 = 6, W2_N3 = 8;
static constexpr int W2_N5 = 12, W2_N6 = 4;
static constexpr int W2_S1 = 0, W2_S0 = W2_S1 + W2_N1, W2_S2 = W2_S0 + W2_N0, W2_S2B = W2_S2 + W2_N2, W2_S4 = W2_S2B + W2_N2B,
                     W2_S3 = W2_S4 + W2_N4, W2_S5 = W2_S3 + W2_N3, W2_S6 = W2_S5 + W2_N5 / 2, W2_S5B = W2_S6 + W2_N6;
static_assert(W2_S5B + W2_N5 / 2 == 56, "all of w2");
static constexpr int PA_AT = 24, W2_AFTER = W2_N5 / 2 - ((PA_AT - 15) * W2_N5) / 32;
static constexpr int PF0 = 4;
static constexpr int DEP = 8, G2_PL = 7;

static constexpr int NW2A = 16;
struct W2F { u32x4 a[28], b[28]; u32 so, vo[4]; };
template <int N> DI void w2_load(W2F& f, rsrc_t rs) {
  constexpr int k = N >> 1;
  const u32 so = f.so + (N >> 2) * 4096;
  if constexpr (k < NW2A) { if constexpr (N & 1) BLD_A(f.b[k], f.vo[N & 3], so, 0); else BLD_A(f.a[k], f.vo[N & 3], so, 0); }
  else { if constexpr (N & 1) BLD_V(f.b[k], f.vo[N & 3], so, 0); else BLD_V(f.a[k], f.vo[N & 3], so, 0); }
}
template <int N0, int N1> DI void w2_range(W2F& f, rsrc_t rs) {
  if constexpr (N0 < N1) { w2_load<N0>(f, rs); w2_range<N0 + 1, N1>(f, rs); }
}
template <int N, int NE> DI void idle_w2(W2F& f, u32x4& dummy, rsrc_t rs, u32 voff) {
  if constexpr (N < NE) {
    asm volatile("buffer_load_dwordx4 %0, %1, %2, %3 offen offset:%4 sc1"
                 : "+v"(dummy) : "v"(voff), "s"(rs), "s"(f.so - 4 * 56 * 4096 + (N >> 2) * 4096), "i"((N & 3) * 1024) : "memory");
    w2_load<N>(f, rs);
    idle_w2<N + 1, NE>(f, dummy, rs, voff);
  }
}

struct G1 {
  u32x4 wf[32];
  f32x2 cs[8], bb[8];
  float cp[9];
  u32 ta[2][2];
  u32 xw, xr, cbr, cbw, spw, spr, fvo, avo, rvo, fdv, fd_vo;
  u32 hso, hdst, rso, rdst, pso, fdo, fda;
  u64 tag, pl;
  float t0, t1, nmu, rl;
};
template <int M, int P> DI u32x4 hfrag(const G1& g) {
  return cat(tr_read(g.ta[M >> 1][P >> 3] + (M & 1) * 32768 + (P & 7) * 1024), tr_read(g.ta[M >> 1][P >> 3] + (M & 1) * 32768 + (P & 7) * 1024 + 256));
}
template <int J> DI void stat_step(G1& g, f32x2 (&sq)[4]) {
  if constexpr (J == 19) asm volatile("" : "+v"(sq[0][0]), "+v"(sq[1][0]), "+v"(sq[2][0]), "+v"(sq[3][0]), "+v"(sq[0][1]), "+v"(sq[1][1]), "+v"(sq[2][1]), "+v"(sq[3][1]));
  if constexpr (J == 20) { g.nmu = sq[0][0] + sq[1][0]; g.t0 = sq[2][0] + sq[3][0]; }
  if constexpr (J == 21) { g.rl = sq[0][1] + sq[1][1]; g.t1 = sq[2][1] + sq[3][1]; asm volatile("" : "+v"(g.nmu), "+v"(g.t0)); }
  if constexpr (J == 22) g.nmu = (g.nmu + g.t0) * (-1.f / SD);
  if constexpr (J == 23) g.rl = __builtin_fmaf(g.rl + g.t1, 1.f / SD, 1e-5f);
  if constexpr (J == 24) g.rl = fmaxf(__builtin_fmaf(-g.nmu, g.nmu, g.rl), 1e-5f);
  if constexpr (J == 25) g.rl = __builtin_amdgcn_rsqf(g.rl);
  if constexpr (J == 27) g.rl *= LOG2E;
}
template <int Q> DI void h_piece(rsrc_t rs, u32 voff, u32 so, u32 dst) {
  if constexpr ((Q & 3) == 0) BLDS1(voff, so + (Q >> 2) * 4096, dst + (Q >> 2) * 4096);
  else BLDS1N(voff, so + (Q >> 2) * 4096, (Q & 3) * 1024);
}
template <int M, int J, int JE> struct Gap {
  static DI void run(G1& g, W2F& w2, f32x16& acc, f32x16& snd, u32x4 (&fr)[16], f32x2 (&gv)[8], f32x2 (&ev)[8], float (&es)[16], f32x2 (&sq)[4], rsrc_t rs, u32 voff) {
    if constexpr (J < JE) {
      constexpr bool S = J < 20 ? (J & 1) == 0 : J < 26;
      constexpr int P = J < 20 ? J >> 1 : J < 26 ? J - 10 : J - 16;
      if constexpr (J == 0) MFMA_AV0(snd, g.wf[16], fr[0]);
      else if constexpr (J == 1) MFMA_AV0(acc, g.wf[0], fr[0]);
      else if constexpr (S && J >= 20 && (J & 1) == 0) MFMA_AV2(snd, g.wf[16 + P], fr[P], fr[P + 1]);
      else if constexpr (S) MFMA_AV(snd, g.wf[16 + P], fr[P]);
      else MFMA_AV(acc, g.wf[P], fr[P]);
      if constexpr (J < (M == 0 ? 10 : 14) && (J & 1) == 0) fr[J / 2 + 3] = hfrag<M, J / 2 + 3>(g);
      if constexpr (J >= (M == 0 ? 16 : 14) && J < 20) fr[J - 4] = hfrag<M, J - 4>(g);
      if constexpr (J == 28 && M == 0) asm volatile("s_nop 3" : "+v"(snd));
      if constexpr (J == 28 && M > 0) asm volatile("" : "+v"(snd));
      if constexpr (J >= 28) lds_st(g.xw + (J - 28) * 1024, f32x4{snd[4 * (J - 28)], snd[4 * (J - 28) + 1], snd[4 * (J - 28) + 2], snd[4 * (J - 28) + 3]});
      if constexpr (M == 0 && J < 16 && (J & 1) == 0) h_piece<J / 2>(rs, voff, g.hso + HT_S, g.hdst + L_HE + 32768);
      if constexpr (M == 0 && J >= 16) w2_range<W2_S0 + ((J - 16) * W2_N0) / 16, W2_S0 + ((J - 15) * W2_N0) / 16>(w2, rs);
      if constexpr (M == 0 && J >= 20 && J < 24) {
        const f32x4 t = lds_ld<f32x4>(g.cbr + (J - 20) * 32), u = lds_ld<f32x4>(g.cbr + 512 + (J - 20) * 32);
        g.cs[2 * (J - 20)] = f32x2{t[0], t[1]}; g.cs[2 * (J - 20) + 1] = f32x2{t[2], t[3]}; g.bb[2 * (J - 20)] = f32x2{u[0], u[1]}; g.bb[2 * (J - 20) + 1] = f32x2{u[2], u[3]};
      }
      if constexpr (M == 1 && J < 8) w2_range<W2_S2 + (J * W2_N2) / 8, W2_S2 + ((J + 1) * W2_N2) / 8>(w2, rs);
      if constexpr (M == 1 && J == 9) g.pl = poll_issue(rs, OFF_FC2 + g.pso, voff);
      if constexpr (M == 1 && J >= 16 && (J & 1) == 0) h_piece<(J - 16) / 2>(rs, voff, g.hso + 32 * HT_S, g.hdst + L_HL);
      if constexpr (M == 2 && J < 16 && (J & 1) == 0) h_piece<J / 2>(rs, voff, g.hso + 33 * HT_S, g.hdst + L_HL + 32768);
      if constexpr (M == 2 && J == 15) { BLDS1(g.rvo, g.rso, g.rdst); BLDS1(g.rvo, g.rso + HT_S, g.rdst + 2048); }
      if constexpr (M == 2 && J >= 16) w2_range<W2_S3 + ((J - 16) * W2_N3) / 16, W2_S3 + ((J - 15) * W2_N3) / 16>(w2, rs);
      if constexpr (M == 3 && J < 16) w2_range<W2_S5 + (J * W2_N5) / 32, W2_S5 + ((J + 1) * W2_N5) / 32>(w2, rs);
      if constexpr (M == 3 && J >= 16) w2_range<W2_S5B + ((J - 16) * W2_N5) / 32, W2_S5B + ((J - 15) * W2_N5) / 32>(w2, rs);
      if constexpr (M == 3 && J == PA_AT) g.pl = poll_issue(rs, g.fda, g.fd_vo);
      stat_step<J>(g, sq);
      if constexpr (M > 0) {
        if constexpr (J < 16) {
          if constexpr (J > 0) asm volatile("v_add_f32 %0, 1.0, %0" : "+v"(es[J - 1]));
          asm volatile("v_exp_f32 %0, %1" : "=v"(es[J]) : "v"(gv[J >> 1][J & 1]));
        } else {
          asm volatile("v_rcp_f32 %0, %1" : "=v"(ev[(J - 16) >> 1][J & 1]) : "v"(es[J - 16]));
          if constexpr (J < 31) asm volatile("v_fma_f32 %0, %0, %0, 1.0" : "+v"(es[J - 15]));
        }
      }
      SB();
      Gap<M, J + 1, JE>::run(g, w2, acc, snd, fr, gv, ev, es, sq, rs, voff);
    }
  }
};
template <int M, int J> struct Dot {
  static DI void run(W2F& w2, const u32x4 (&fr)[16], float& s1, float& s2, rsrc_t rs) {
    if constexpr (J < 8) {
      constexpr int S = M == 0 ? W2_S1 : M == 1 ? W2_S2B : M == 2 ? W2_S4 : W2_S6, N = M == 0 ? W2_N1 : M == 1 ? W2_N2B : M == 2 ? W2_N4 : W2_N6;
      w2_range<S + (J * N) / 8, S + ((J + 1) * N) / 8>(w2, rs);
#pragma unroll
      for (int e = 0; e < 4; ++e) { s1 = dot2bf(fr[J][e], 0x3f803f80u, s1); s2 = dot2bf(fr[J][e], fr[J][e], s2); }
      SB();
      Dot<M, J + 1>::run(w2, fr, s1, s2, rs);
    }
  }
};
#define EACH8(x) _Pragma("unroll") for (int j = 0; j < 8; ++j) { x; } SB()
template <int M> DI void g1_tile(G1& g, W2F& w2, f32x16& acc, f32x16& prev, rsrc_t rs, u32 voff, u32 act_so) {
  u32x4 fr[16];
  f32x2 gv[8], ev[8], sq[4];
  f32x16 snd;
  float es[16];
  if constexpr (M > 0) {
    asm volatile("s_waitcnt vmcnt(%0)" ::"i"(M == 1 ? W2_N1 + 1 + W2_N0 : M == 2 ? 2 : W2_N4 + 1 + W2_N3 + 2) : "memory");
    fr[0] = hfrag<M, 0>(g); fr[1] = hfrag<M, 1>(g); fr[2] = hfrag<M, 2>(g);
    if constexpr (M == 1) asm volatile("s_nop 3" : "+v"(prev));
    SB();
    EACH8(gv[j] = g.cs[j] * g.nmu + (f32x2{prev[2 * j], prev[2 * j + 1]}));
    bar();
    f32x4 px[4];
#pragma unroll
    for (int q = 0; q < 4; ++q) px[q] = lds_ld<f32x4>(g.xr + q * 1024);
    SB();
    EACH8(gv[j] += (f32x2{px[j >> 1][2 * (j & 1)], px[j >> 1][2 * (j & 1) + 1]}));
    EACH8(gv[j] = gv[j] * g.rl + g.bb[j]);
  } else {
    fr[0] = hfrag<M, 0>(g); fr[1] = hfrag<M, 1>(g); fr[2] = hfrag<M, 2>(g);
    SB();
  }
  Gap<M, 0, 16>::run(g, w2, acc, snd, fr, gv, ev, es, sq, rs, voff);
  if constexpr (M == 0) {
    asm volatile("s_waitcnt vmcnt(8)" : "+v"(g.cp[0]), "+v"(g.cp[1]), "+v"(g.cp[2]), "+v"(g.cp[3]), "+v"(g.cp[4]), "+v"(g.cp[5]), "+v"(g.cp[6]), "+v"(g.cp[7]), "+v"(g.cp[8]) :: "memory");
    lds_st<float>(g.cbw, ((g.cp[0] + g.cp[1]) + (g.cp[2] + g.cp[3])) + ((g.cp[4] + g.cp[5]) + (g.cp[6] + g.cp[7])));
    lds_st<float>(g.cbw + 512, bflo(__float_as_uint(g.cp[8])) * LOG2E);
    SB();
  }
  float s1 = 0.f, s2 = 0.f;
  Dot<M, 0>::run(w2, fr, s1, s2, rs);
  lds_st<float>(g.spw + (M & 1) * 1024, fold32(s1, s2));
  if constexpr (M > 0) asm volatile("v_add_f32 %0, 1.0, %0\n\tv_fma_f32 %1, %1, %1, 1.0" : "+v"(es[15]), "+v"(es[0]));
  if constexpr (M == 1) poll_wait<W2_N2B>(g.pl, rs, OFF_FC2 + g.pso, voff, g.tag);
  asm volatile("s_waitcnt vmcnt(%0) lgkmcnt(0)\n\ts_barrier" ::"i"(M < 2 ? 63 : M == 2 ? 10 + W2_N4 : W2_N5 / 2 + W2_N6) : "memory");
  if constexpr (M >= 2) flag_st(rs, g.fdv, g.fdo + (M - 2) * FD_T, g.tag);
  if constexpr (M == 0) {
    flag_st(rs, g.fvo, OFF_FC2, g.tag);
    fr[8] = hfrag<M, 8>(g); fr[9] = hfrag<M, 9>(g); fr[10] = hfrag<M, 10>(g); fr[11] = hfrag<M, 11>(g);
  }
#pragma unroll
  for (int q = 0; q < 4; ++q) sq[q] = lds_ld<f32x2>(g.spr + (M & 1) * 1024 + q * 256);
  SB();
  Gap<M, 16, 32>::run(g, w2, acc, snd, fr, gv, ev, es, sq, rs, voff);
  if constexpr (M > 0) {
    u32 ov[8];
    EACH8(ev[j] = ev[j] * (-2.f * LN2) + LN2);
    EACH8(gv[j] *= ev[j]);
#pragma unroll
    for (int j = 0; j < 8; ++j) ov[j] = pk_bf16(gv[j][0], gv[j][1]);
    const u32 so = act_so + (((M - 1) & 1) + 32 * ((M - 1) >> 1)) * (112 * 1024);
    st16(rs, g.avo, so, u32x4{ov[0], ov[1], ov[2], ov[3]});
    st16(rs, g.avo, so + 512, u32x4{ov[4], ov[5], ov[6], ov[7]});
  } else {
    asm volatile("s_nop 4" : "+v"(acc));
  }
  SB();
}

static_assert(DEP % 2 == 0 && DEP <= 14, "step DEP waits");
constexpr bool g2_poll(int k) { return k == 28 - DEP - G2_PL || k == 42 - DEP - G2_PL; }
constexpr bool g2_store(int k) { return k >= 14 && k % 14 == 12; }
constexpr bool g2_flag(int k) { return k == 0 || k == DEP; }
constexpr int g2_ops(int k) { return (k + DEP < 56 ? 1 : 0) + (g2_poll(k) ? 1 : 0) + (g2_store(k) ? 1 : 0) + (g2_flag(k) ? 1 : 0); }
constexpr int g2_span(int a, int b) { int n = 0; for (int k = a; k < b; ++k) n += g2_ops(k); return n; }
constexpr int g2_younger(int k) { return k < DEP ? DEP - 1 - k + 3 + g2_span(0, k) : g2_span(k - DEP, k) - 1; }
constexpr int g2_tile(int m) { return (m & 1) + 32 * (m >> 1); }
struct G2 {
  u32x4 af[56], hr[4], id;
  f32x4 acc[2][4], pv[4];
  u32 aso, fda, fd_vo, wp[4], rq, oo, o2[2], fl_vo[2], fl_so[2];
  u64 pl, tag;
};
#define MF16(c, a, b) "v_mfma_f32_16x16x32_bf16 " c ", " a ", " b
template <int N, bool Z> DI void g2_mfma(f32x4& acc, const W2F& f, const u32x4& b) {
  const u32x4& a = (N & 1) ? f.b[N >> 1] : f.a[N >> 1];
  if constexpr (Z) asm volatile(MF16("%0", "%1", "%2") ", 0" : "=&v"(acc) : "a"(a), "a"(b));
  else if constexpr (N < 2 * NW2A) asm volatile(MF16("%0", "%1", "%2") ", %0" : "+v"(acc) : "a"(a), "a"(b));
  else asm volatile(MF16("%0", "%1", "%2") ", %0" : "+v"(acc) : "v"(a), "a"(b));
}
template <int K, int KE> DI void g2_pro(G2& e, rsrc_t rs, u32 voff) {
  if constexpr (K < KE) { BLD_A(e.af[K], voff, e.aso + (K >> 2) * 4096, (K & 3) * 1024); g2_pro<K + 1, KE>(e, rs, voff); }
}
#define G2H(W, C1, C2, LD) W MF16("%0", "%3", "%5") C1 "\n\t" LD MF16("%1", "%4", "%5") C2
#define G2W "s_waitcnt vmcnt(%10)\n\t"
#define G2L "buffer_load_dwordx4 %2, %6, %7, %8 offen offset:%9 sc1\n\t"
#define G2ASM(T, O, A) asm volatile(T : O(acc[1]), O(acc[2]), "=&a"(e.af[L]) : A(a1), A(a2), "a"(e.af[K]), "v"(voff), "s"(rs), "s"(so), "i"((lk & 3) * 1024), "i"(WT < 0 ? 0 : WT) : "memory")
#define G2ASN(T, O, A) asm volatile(T : O(acc[1]), O(acc[2]), "=s"(none) : A(a1), A(a2), "a"(e.af[K]), "i"(0), "i"(0), "i"(0), "i"(0), "i"(WT < 0 ? 0 : WT) : "memory")
template <int K, int WT> DI void g2_head(G2& e, f32x4 (&acc)[4], const W2F& f, rsrc_t rs, u32 voff) {
  constexpr int k = K % 14, L = K + DEP < 56 ? K + DEP : 0, lm = L / 14, lk = L % 14;
  const u32x4 &a1 = f.b[2 * k], &a2 = f.a[2 * k + 1];
  const u32 so = e.aso + g2_tile(lm) * (112 * 1024) + (lk >> 2) * 4096;
  u32 none;
  if constexpr (K + DEP < 56) {
    if constexpr (k == 0) G2ASM(G2H(G2W, ", 0", ", 0", G2L), "=&v", "a");
    else if constexpr (2 * k < NW2A && WT >= 0) G2ASM(G2H(G2W, ", %0", ", %1", G2L), "+v", "a");
    else if constexpr (2 * k < NW2A) G2ASM(G2H("", ", %0", ", %1", G2L), "+v", "a");
    else if constexpr (WT >= 0) G2ASM(G2H(G2W, ", %0", ", %1", G2L), "+v", "v");
    else G2ASM(G2H("", ", %0", ", %1", G2L), "+v", "v");
  } else {
    static_assert(k > 0, "");
    if constexpr (2 * k < NW2A && WT >= 0) G2ASN(G2H(G2W, ", %0", ", %1", ""), "+v", "a");
    else if constexpr (2 * k < NW2A) G2ASN(G2H("", ", %0", ", %1", ""), "+v", "a");
    else if constexpr (WT >= 0) G2ASN(G2H(G2W, ", %0", ", %1", ""), "+v", "v");
    else G2ASN(G2H("", ", %0", ", %1", ""), "+v", "v");
  }
}
template <int m, int G> DI void g2_fill(G2& e, f32x4 (&prv)[4], rsrc_t ro) {
  if constexpr (m > 0) {
    constexpr int o = ((m - 1) & 1) * 17408;
    if constexpr (G == 3) asm volatile("" : "+v"(prv[0]), "+v"(prv[1]), "+v"(prv[2]), "+v"(prv[3]));
    if constexpr (G >= 3 && G < 7) lds_st(e.wp[G - 3] + o, prv[G - 3]);
    if constexpr (G == 10) bar();
    if constexpr (G >= 12 && G < 16) e.pv[G - 12] = lds_ld<f32x4>(e.rq + o + (G - 12) * 4352);
    if constexpr (G >= 19 && G < 31) asm volatile("v_add_f32 %0, %0, %1" : "+v"(e.pv[0][(G - 19) & 3]) : "v"(e.pv[1 + ((G - 19) >> 2)][(G - 19) & 3]));
    if constexpr (G == 32 || G == 33) asm volatile("v_cvt_pk_bf16_f32 %0, %1, %2" : "=v"(e.o2[G - 32]) : "v"(e.pv[0][2 * (G - 32)]), "v"(e.pv[0][2 * (G - 32) + 1]));
    if constexpr (G == 36) __builtin_amdgcn_raw_buffer_store_b64(u32x2{e.o2[0], e.o2[1]}, ro, e.oo, g2_tile(m - 1) * 32768, 16);
  }
}
template <int K> struct G2S {
  static DI void run(G2& e, const W2F& f, rsrc_t rs, rsrc_t ro, u32 voff) {
    if constexpr (K < 56) {
      constexpr int m = K / 14, k = K % 14, nx = (K + DEP) / 14;
      f32x4 (&acc)[4] = e.acc[m & 1];
      f32x4 (&prv)[4] = e.acc[(m & 1) ^ 1];
      if constexpr (K + DEP < 56 && (K + DEP) % 14 == 0)
        poll_wait<(nx == 1 ? g2_span(0, K) : g2_span(K - G2_PL + 1, K))>(e.pl, rs, e.fda + g2_tile(nx) * FD_T, e.fd_vo, e.tag);
      if constexpr (k == 0) asm volatile(MF16("%0", "%1", "%2") ", 0" : "=&v"(acc[0]) : "v"(e.id), "v"(e.hr[m]));
      g2_head<K, ((K & 1) ? -1 : g2_younger(K + 1) - g2_ops(K))>(e, acc, f, rs, voff);
      if constexpr (g2_flag(K)) { asm volatile("s_barrier" ::: "memory"); flag_st(rs, e.fl_vo[K > 0], e.fl_so[K > 0], e.tag); }
      if constexpr (g2_poll(K)) e.pl = poll_issue(rs, e.fda + g2_tile(nx + 1) * FD_T, e.fd_vo);
      g2_fill<m, 3 * k>(e, prv, ro);
      SB();
      g2_mfma<4 * k + 3, k == 0>(acc[3], f, e.af[K]);
      g2_fill<m, 3 * k + 1>(e, prv, ro);
      SB();
      g2_mfma<4 * k, false>(acc[0], f, e.af[K]);
      g2_fill<m, 3 * k + 2>(e, prv, ro);
      SB();
      G2S<K + 1>::run(e, f, rs, ro, voff);
    }
  }
};

__global__ __launch_bounds__(256, 1) void s4_mlp(const u16* __restrict__ x, const u16* __restrict__ Are, const u16* __restrict__ Aim,
                                                 const u16* __restrict__ Bre, const u16* __restrict__ Bim, const u16* __restrict__ Cre,
                                                 const u16* __restrict__ Cim, const u16* __restrict__ logdt, const u16* __restrict__ dskip,
                                                 const u16* __restrict__ w1, const u16* __restrict__ b1, const u16* __restrict__ w2,
                                                 u16* __restrict__ out, char* ws) {
  __shared__ __attribute__((aligned(16))) char smem[L_END];
  const u32 lds0 = (u32)(size_t)(LDSP(char))smem;
  const int b = blockIdx.x, tid = threadIdx.x, lane = tid & 63;
  const int w = __builtin_amdgcn_readfirstlane(tid >> 6);
  const int xg = b & 7, bi = b >> 3, r = lane & 31, h = lane >> 5;
  const rsrc_t rs = __builtin_amdgcn_make_buffer_rsrc(ws, 0, WS_BYTES, 0x00020000);
  const u32 voff = lane * 16;
  const u32 xo = (8 * b + 2 * w) * 1024 + voff;
  const u32x4 xr0 = *(const u32x4*)((const char*)x + xo), xr1 = *(const u32x4*)((const char*)x + 1024 + xo);
  SB();
  const int d0 = 64 * xg + 2 * bi, c = w >> 1, hf = w & 1, d = d0 + c;
  u32 pf_dummy = 0, pt_dummy = 0;
  if (bi < 4 && w == 3) {
    u64 pc; u32 len;
    asm volatile("s_getpc_b64 %0\n.Ls4_pc:\n\ts_mov_b32 %1, .Ls4_end - .Ls4_pc" : "=s"(pc), "=s"(len));
    asm volatile("global_load_dword %0, %1, %2" : "=v"(pf_dummy) : "v"(min((u32)((bi * 64 + lane) * 128), len)), "s"(pc) : "memory");
  }
  const u32x2 gen = __builtin_amdgcn_raw_buffer_load_b64(rs, 0, OFF_GEN, 16);
  if (w == 2) {
    const u32 fdl = ((4 * xg + (bi >> 3)) + (lane & 16) * 2) * FD_T + min((u32)(lane & 15) * 128, FD_T - 128);
    const u32 pg = lane >= 16 ? (lane - 16) * (2u << 20) + 49152 : OOB;
    const u32 o = lane < 4 ? OFF_FA + lane * 32768 + xg * 4096 + bi * 128 : lane == 8 ? OFF_ST1 + b * 64 : lane >= 32 ? OFF_FD + fdl : pg;
    asm volatile("buffer_load_dword %0, %1, %2, 0 offen sc1" : "=v"(pt_dummy) : "v"(o), "s"(rs) : "memory");
  }
  const u32 po = (d * SN + hf * 64 + lane) * 2;
#define PARAM(p) (*(const u16*)((const char*)(p) + po))
  const u16 r_are = PARAM(Are), r_aim = PARAM(Aim), r_bre = PARAM(Bre), r_bim = PARAM(Bim), r_cre = PARAM(Cre), r_cim = PARAM(Cim);
  const u16 r_ldt = *(const u16*)((const char*)logdt + opq(d * 2));
  const u32 dsk2 = *(const u32*)((const char*)dskip + opq(d0 * 2));
  u32x4 ga[3];
#pragma unroll
  for (int j = 0; j < 3; ++j) ga[j] = *(const u32x4*)((const char*)gather_tab.v + 4096 + (u32)(tid * 16) + (j - 1) * 4096);
  const int bc = min(bi, 27);
  {
    const u32x4 z4 = {0u, 0u, 0u, 0u};
    const u32 a = lds0 + L_U + min(tid, 154) * 16;
    lds_st(a, z4); lds_st(a + 7600, z4);
    lds_st(lds0 + L_R + ((tid >> 2) & 7) * 4160 + 4096 + (tid & 3) * 16, z4);
    lds_st<u32>(lds0 + L_CMP + 32 * 112, 0u);
  }
  const u32 fvo = tid < 8 ? tid * 4096 + b * 16 : OOB;
  const u32 pso = xg * 4096 + w * 1024;
  const u32 trq = ((lane & 15) >> 2), trp = (lane & 3) * 8 + (lane & 16) * 2;
  lds_st(lds0 + L_LN + w * 2176 + voff, xr0);
  lds_st(lds0 + L_LN + w * 2176 + 1088 + voff, xr1);
  bar();
#pragma unroll
  for (int k = 0; k < 2; ++k) {
    const u32 p = lds0 + L_LN + trq * 1088 + w * 256 + k * 128 + h * 64 + trp;
    st16(rs, (lane >> 2) * XT_S + (lane & 3) * 16, OFF_XT + (32 * w + 16 * k) * XT_S + b * 64, cat(tr_read(p), tr_read(p + 4 * 1088)));
  }
  {
    float a0 = 0.f, a1 = 0.f, q0 = 0.f, q1 = 0.f;
#pragma unroll
    for (int j = 0; j < 4; ++j) {
      a0 = dot2bf(xr0[j], 0x3f803f80u, a0); q0 = dot2bf(xr0[j], xr0[j], q0);
      a1 = dot2bf(xr1[j], 0x3f803f80u, a1); q1 = dot2bf(xr1[j], xr1[j], q1);
    }
    const float mean = sum32(fold32(a0, a1)) * (1.f / SD);
    const float var = fmaxf(sum32(fold32(q0, q1)) * (1.f / SD) - mean * mean, 0.f);
    const float ri = __builtin_amdgcn_rcpf(rbf(__builtin_amdgcn_sqrtf(rbf(rbf(var) + 1e-5f))));
    st4(rs, r < 2 ? r * 32 + h * 4 : OOB, OFF_ST1 + b * 64 + w * 8, __float_as_uint(r == 0 ? -rbf(mean) * ri : ri));
  }
  const u32 wdst = lds0 + L_WS + w * 4096;
  {
    const u32 v1 = (64 * xg + 16 * w + (lane >> 3)) * (SF * 2) + 128 * bc + (lane & 7) * 16;
    GLDS1(v1, w1, wdst); GLDS1(v1 + 8 * SF * 2, w1, wdst + 1024);
  }
  const u64 tag = ((((u64)(u32)__builtin_amdgcn_readfirstlane(gen[1])) << 32) | (u32)__builtin_amdgcn_readfirstlane(gen[0])) + 0x9e3779b97f4a7c15ull;
  float scale;
  {
    const float dt = rbf(__builtin_amdgcn_exp2f(bf2f(r_ldt) * LOG2E));
    const float a = dt * bf2f(r_are) * LOG2E, bq = dt * bf2f(r_aim) * 0.15915494309189535f;
    const float dbr = dt * bf2f(r_bre), dbi = dt * bf2f(r_bim), cre = bf2f(r_cre), cim = bf2f(r_cim);
    const float wr = cre * dbr - cim * dbi, wi = cre * dbi + cim * dbr;
    u32 sb = wave_max(__float_as_uint(fmaxf(fabsf(wr), fabsf(wi)) * __builtin_amdgcn_exp2f(fmaxf(a, 0.f) * 2048.f)));
    sb = min(max((sb + 0x007fffffu) & 0x7f800000u, 0x00800000u), 0x7e800000u);
    scale = __uint_as_float(sb);
    const u32 ef = opq(lds0 + L_EF + w * (53 * 272) + lane * 4);
    f32x2 T[53];
    T[0] = f32x2{wr, wi} * __uint_as_float(0x7f000000u - sb);
    T[21] = f32x2{1.f, 0.f};
    T[22] = cexp_rev(a * 8.f, bq * -8.f);
    const Cm f1 = cm(T[22]), m1 = cm(cexp_rev(a, bq)), m256 = cm(cexp_rev(a * 256.f, bq * 256.f));
    lds_st<u32>(ef, pk_f16(T[0][0], T[0][1])); lds_st<u32>(ef + 21 * 272, 0x3c00u); lds_st<u32>(ef + 22 * 272, pk_f16(T[22][0], T[22][1]));
    f32x2 t[16], w2c, w4c;
#define CB1(n, dst, src, m) t[n] = (m).s * (src)[1]
#define CB2(n, dst, src, m) dst = (m).a * (src)[0] + t[n]
#define PUT(row) lds_st<u32>(ef + (row) * 272, pk_f16(T[row][0], T[row][1]))
#define BATCH(X) X(CB1); SB(); X(CB2); SB()
#define X1(OP) OP(0, T[23], T[22], f1); OP(1, w2c, m1.a, m1); OP(2, T[1], T[0], m1); OP(3, T[8], T[0], m256)
    BATCH(X1);
    const Cm f2 = cm(T[23]), m2 = cm(w2c);
    PUT(23); PUT(1); PUT(8);
#define X2(OP) OP(0, T[24], T[22], f2); OP(1, T[25], T[23], f2); OP(2, w4c, w2c, m2); OP(3, T[12], T[8], m256); OP(4, T[2], T[0], m2); OP(5, T[3], T[1], m2); OP(6, T[9], T[8], m2)
    BATCH(X2);
    const Cm f4 = cm(T[25]), m4 = cm(w4c);
    PUT(24); PUT(25); PUT(12); PUT(2); PUT(3); PUT(9);
#define X3(OP) OP(0, T[29], T[25], f4); OP(1, T[14], T[12], m256); OP(2, T[26], T[22], f4); OP(3, T[27], T[23], f4); OP(4, T[28], T[24], f4); \
               OP(5, T[4], T[0], m4); OP(6, T[5], T[1], m4); OP(7, T[6], T[2], m4); OP(8, T[7], T[3], m4); OP(9, T[10], T[8], m4); OP(10, T[11], T[9], m4); OP(11, T[13], T[12], m4)
    BATCH(X3);
    const Cm f8 = cm(T[29]);
    PUT(26); PUT(27); PUT(28); PUT(29); PUT(14); PUT(4); PUT(5); PUT(6); PUT(7); PUT(10); PUT(11); PUT(13);
#define X4(OP) OP(0, T[37], T[29], f8); OP(1, T[16], T[14], m256); OP(2, T[30], T[22], f8); OP(3, T[31], T[23], f8); OP(4, T[32], T[24], f8); OP(5, T[33], T[25], f8); \
               OP(6, T[34], T[26], f8); OP(7, T[35], T[27], f8); OP(8, T[36], T[28], f8); OP(9, T[15], T[14], m4)
    BATCH(X4);
    const Cm f16 = cm(T[37]);
    PUT(30); PUT(31); PUT(32); PUT(33); PUT(34); PUT(35); PUT(36); PUT(37); PUT(15); PUT(16);
    asm volatile("s_waitcnt vmcnt(2)\n\ts_barrier" ::: "memory");
    flag_st(rs, fvo, OFF_FA, tag);
    {
      const u32 v2 = (64 * bc + 16 * w + (lane >> 3)) * (SD * 2) + 128 * xg + (lane & 7) * 16;
      GLDS1(v2, w2, wdst + 2048); GLDS1(v2 + 8 * SD * 2, w2, wdst + 3072);
    }
#define X5(OP) OP(0, T[17], T[16], m256); OP(1, T[38], T[22], f16); OP(2, T[39], T[23], f16); OP(3, T[40], T[24], f16); OP(4, T[41], T[25], f16)
    BATCH(X5);
    PUT(17); PUT(38); PUT(39); PUT(40); PUT(41);
#define X6(OP) OP(0, T[18], T[17], m256); OP(1, T[42], T[26], f16); OP(2, T[43], T[27], f16); OP(3, T[44], T[28], f16); OP(4, T[45], T[29], f16)
    BATCH(X6);
    PUT(18); PUT(42); PUT(43); PUT(44); PUT(45);
#define X7(OP) OP(0, T[19], T[18], m256); OP(1, T[46], T[30], f16); OP(2, T[47], T[31], f16); OP(3, T[48], T[32], f16); OP(4, T[49], T[33], f16)
    BATCH(X7);
    PUT(19); PUT(46); PUT(47); PUT(48); PUT(49);
#define X8(OP) OP(0, T[20], T[19], m256); OP(1, T[50], T[34], f16); OP(2, T[51], T[35], f16); OP(3, T[52], T[36], f16)
    BATCH(X8);
    PUT(20); PUT(50); PUT(51); PUT(52);
    asm volatile("" ::: "memory");
  }
  {
    const u32 ea = opq(lds0 + L_EF + w * (53 * 272) + min(r, 20) * 272 + h * 16), fa = opq(lds0 + L_EF + w * (53 * 272) + (21 + r) * 272 + h * 16);
    u32x4 ef[16];
#pragma unroll
    for (int st = 0; st < 8; ++st) { ef[2 * st] = lds_ld<u32x4>(ea + st * 32); ef[2 * st + 1] = lds_ld<u32x4>(fa + st * 32); }
    f32x16 kacc;
    MFMA_H0(kacc, ef[0], ef[1]);
#pragma unroll
    for (int st = 1; st < 8; ++st) MFMA_H(kacc, ef[2 * st], ef[2 * st + 1]);
    asm volatile("s_nop 11" : "+v"(kacc));
#pragma unroll
    for (int q = 0; q < 3; ++q)
      lds_st(lds0 + L_PART + w * 3584 + r * 112 + q * 32 + h * 16, f32x4{kacc[4 * q], kacc[4 * q + 1], kacc[4 * q + 2], kacc[4 * q + 3]} * scale);
  }
  bar();
  if (w < 3) {
    const u32 pp = lds0 + L_PART + r * 112 + (tid >> 5) * 16;
    const f32x4 s0 = lds_ld<f32x4>(pp) + lds_ld<f32x4>(pp + 3584), s1 = lds_ld<f32x4>(pp + 7168) + lds_ld<f32x4>(pp + 10752);
    lds_st(pp + (L_CMP - L_PART), u32x4{pk_bf16(s0[0], s1[0]), pk_bf16(s0[1], s1[1]), pk_bf16(s0[2], s1[2]), pk_bf16(s0[3], s1[3])});
  }
  const u32 dup = bi >= 28 ? FAR : 0u;
  {
    const u32 wq = wdst + trq * 128 + trp + h * 1024;
    asm volatile("s_waitcnt vmcnt(3)" ::: "memory");
    const u32x4 f0 = cat(tr_read(wq), tr_read(wq + 512)), f1 = cat(tr_read(wq + 64), tr_read(wq + 64 + 512));
    st16(rs, voff, OFF_W1T + (2 * bc * 32 + 4 * xg + w) * 1024 + dup, f0);
    st16(rs, voff, OFF_W1T + ((2 * bc + 1) * 32 + 4 * xg + w) * 1024 + dup, f1);
    float s0 = 0.f, s1 = 0.f;
#pragma unroll
    for (int e = 0; e < 4; ++e) { s0 = dot2bf(f0[e], 0x3f803f80u, s0); s1 = dot2bf(f1[e], 0x3f803f80u, s1); }
    lds_st<float>(lds0 + L_CS + tid * 4, fold32(s0, s1));
  }
  bar();
  if (w == 3) {
    const u32 a = lds0 + L_CS + lane * 4;
    st4(rs, lane * 4 + dup, OFF_CSP + (xg * SF + 64 * bc) * 4, __float_as_uint((lds_ld<float>(a) + lds_ld<float>(a + 256)) + (lds_ld<float>(a + 512) + lds_ld<float>(a + 768))));
  }
  {
    u32 gq[11];
    asm volatile("" : "+v"(ga[2]));
#pragma unroll
    for (int j = 0; j < 11; ++j) gq[j] = lds_ld<u32>(ga[j >> 2][j & 3] + (lds0 + L_CMP));
#pragma unroll
    for (int cc = 0; cc < 2; ++cc) {
      u32 k[10];
#pragma unroll
      for (int j = 0; j < 10; ++j) k[j] = __builtin_amdgcn_perm(gq[j + 1], gq[j], cc ? 0x07060302u : 0x05040100u);
      const u32 rp = lds0 + L_R + cc * 16640 + tid * 16;
      lds_st(rp, u32x4{k[0], k[2], k[4], k[6]});
      lds_st(rp + 4160, u32x4{k[1], k[3], k[5], k[7]});
      lds_st(rp + 8320, u32x4{k[2], k[4], k[6], k[8]});
      lds_st(rp + 12480, u32x4{k[3], k[5], k[7], k[9]});
    }
  }
  const int p1 = 4 * (xg >> 1) + bc / 7, g14 = 7 * (xg & 1) + bc % 7, ft = 4 * g14 + w;
  const int kg0 = 2 * (w & 1) + (w >> 1);
  G1 g;
  u32 w1so[4];
#pragma unroll
  for (int q = 0; q < 4; ++q) { w1so[q] = OFF_W1T + ((ft ^ (q >> 1)) * 32 + (kg0 ^ (q & 1)) * 8) * 1024; asm volatile("" : "+s"(w1so[q])); }
  const u32 voff2 = opq(voff + 4096);
  const int lb = tid >> 1, cc = tid & 1;
  g.hso = OFF_HT + 2 * p1 * HT_S + kg0 * 8192; g.hdst = lds0 + kg0 * 8192;
  const u32 xvo = opq(lb * 64 + cc * 16), svo = opq(lb * 64);
  const u32 uw = opq(lds0 + L_U + cc * 7600 + (31 + (lb >> 2)) * 80 + (lb & 3) * 16);
  const u32 pp = opq(lds0 + L_RED + cc * 9216 + (lb >> 2) * 144 + (lb & 3) * 32);
  const u32 hvo = opq((lb >> 2) * HT_S + (lb & 3) * 16 + OFF_HT + (d0 + cc) * 64);
  const u32 rp = opq(lds0 + L_RED + w * 4608 + r * 144 + h * 16);
  const u32 gA = opq(lds0 + L_R + c * 16640 + ((3 - r) & 3) * 4160 + 2 * (2047 - r + 8 * h - ((3 - r) & 3)) - 32 * (32 * hf + 30) - 2048), gB = opq(gA + 8);
  const u32 up = opq(lds0 + L_U + c * 7600 + (16 + r - 16 * hf) * 80 + 16 * h);
  const float dsk = opqf(cc ? bfhi(dsk2) : bflo(dsk2));
  u32 xso = OFF_XT + (d0 >> 2) * XT_S + (d0 & 3) * 16, w2to = OFF_W2T + (xg * 56 + 2 * bc + (w >> 1)) * 4096 + (w & 1) * 512 + dup;
  asm volatile("" : "+s"(xso), "+s"(w2to));
  poll_wait<0>(poll_issue(rs, OFF_FA + pso, voff), rs, OFF_FA + pso, voff, tag);
  bar();
  u32x4 xv[2], sv[2][4];
#pragma unroll
  for (int k = 0; k < 2; ++k) {
    xv[k] = ld16(rs, xvo, xso + k * 8192);
#pragma unroll
    for (int j = 0; j < 4; ++j) sv[k][j] = ld16(rs, svo + j * 16, OFF_ST1 + k * 8192);
  }
  flag_st(rs, fvo, OFF_FB, tag);
  asm volatile("" ::"v"(pf_dummy));
  asm volatile("" ::"v"(pt_dummy));
  u32x4 wt[2];
  const u32 wq2 = wdst + 2048 + (((lane >> 4) & 1) * 4 + trq) * 128 + h * 32 + (lane & 3) * 8, w2vo = h * 1024 + (lane & 31) * 16;
#pragma unroll
  for (int n = 0; n < 2; ++n) wt[n] = cat(tr_read(wq2 + n * 64), tr_read(wq2 + n * 64 + 1024));
#define OG(n) (32 * (30 - 2 * ((n) >> 1) + ((n) & 1)))
#define OU(n) (80 * (15 - ((n) >> 1)) + 32 * ((n) & 1))
  u32x2 l0[32], l1[32];
#pragma unroll
  for (int n = 0; n < 32; ++n) { l0[n] = lds_ld<u32x2>(gA + 2048 + OG(n)); l1[n] = lds_ld<u32x2>(gB + 2048 + OG(n)); SB(); }
  f32x2 xf[8], uf[8];
  {
    u32x4 ub;
#pragma unroll
    for (int j = 0; j < 4; ++j) {
      xf[j] = f32x2{bflo(xv[0][j]), bfhi(xv[0][j])};
      const u32x4 mm = sv[0][j >> 1], rr = sv[0][2 + (j >> 1)];
      uf[j] = xf[j] * f32x2{__uint_as_float(rr[2 * (j & 1)]), __uint_as_float(rr[2 * (j & 1) + 1])} + f32x2{__uint_as_float(mm[2 * (j & 1)]), __uint_as_float(mm[2 * (j & 1) + 1])};
      ub[j] = pk_bf16(uf[j][0], uf[j][1]);
    }
    lds_st(uw, ub);
  }
  bar();
  float base[16];
  auto conv_out = [&](int k) {
    const f32x4 t0 = lds_ld<f32x4>(pp) + lds_ld<f32x4>(pp + 4608), t1 = lds_ld<f32x4>(pp + 16) + lds_ld<f32x4>(pp + 4608 + 16);
    const float* bs = base + 8 * k;
    st16(rs, hvo, k * 32 * HT_S,
         u32x4{pk_bf16(bs[0] + t0[0], bs[1] + t0[1]), pk_bf16(bs[2] + t0[2], bs[3] + t0[3]), pk_bf16(bs[4] + t1[0], bs[5] + t1[1]), pk_bf16(bs[6] + t1[2], bs[7] + t1[3])});
  };
  {
    u32x2 h0[32], h1[32];
    u32x4 b0[32], b1v[32];
    f32x16 acc;
    u64 pq;
#define B0LD(n) asm volatile("ds_read_b128 %0, %1 offset:%2" : "=a"(b0[n]) : "v"(up), "i"(OU(n)) : "memory")
#pragma unroll
    for (int n = 0; n < 4; ++n) { B0LD(n); SB(); }
#pragma unroll
    for (int n = 0; n < 32; ++n) {
      const u32x4 glo = cat(l0[n], l1[n]);
      if (n == 0) asm volatile("s_waitcnt lgkmcnt(3)\n\t" MF32 "0" : "=&v"(acc) : "v"(glo), "a"(b0[n]) : "memory");
      else asm volatile("s_waitcnt lgkmcnt(%3)\n\t" MF32 "%0" : "+v"(acc) : "v"(glo), "a"(b0[n]), "i"(n < 29 ? 3 : 31 - n + 3 * (n - 29)) : "memory");
      if (n + 4 < 32) B0LD(n + 4);
      if (n >= 2 && n < 10) {
        const int e = n - 2;
        const float xe = opqf((e & 1) ? bfhi(xv[1][e >> 1]) : bflo(xv[1][e >> 1]));
        xf[4 + (e >> 1)][e & 1] = xe;
        uf[4 + (e >> 1)][e & 1] = opqf(__builtin_fmaf(xe, __uint_as_float(sv[1][2 + (e >> 2)][e & 3]), __uint_as_float(sv[1][e >> 2][e & 3])));
      }
      if (n == 10) lds_st(uw + 2560, u32x4{pk_bf16(uf[4][0], uf[4][1]), pk_bf16(uf[5][0], uf[5][1]), pk_bf16(uf[6][0], uf[6][1]), pk_bf16(uf[7][0], uf[7][1])});
      if (n == 20) bar();
      if (n >= 11 && n < 27) {
        const int e = n - 11;
        base[e] = __builtin_fmaf(dsk, uf[e >> 1][e & 1], xf[e >> 1][e & 1]);
        asm volatile("" : "+v"(base[e]));
      }
      if (n == W2T_AT || n == W2T_AT + 1) st16(rs, w2vo, w2to + (n - W2T_AT) * 2048, wt[n - W2T_AT]);
      if (n == FB_POLL) pq = poll_issue(rs, OFF_FB + pso, voff);
      if (n >= 29) {
        const int q = n - 29;
        b1v[q] = lds_ld<u32x4>(up + OU(q) + 2560); h0[q] = lds_ld<u32x2>(gA + OG(q)); h1[q] = lds_ld<u32x2>(gB + OG(q));
      }
      SB();
    }
    asm volatile("s_nop 8" : "+v"(acc));
#pragma unroll
    for (int q = 0; q < 4; ++q) lds_st(rp + q * 32, f32x4{acc[4 * q], acc[4 * q + 1], acc[4 * q + 2], acc[4 * q + 3]});
    poll_wait<0>(pq, rs, OFF_FB + pso, voff, tag);
    bar();
    conv_out(0);
    SB();
#pragma unroll
    for (int n = 0; n < 32; ++n) {
      const u32x4 glo = cat(l0[n], l1[n]), ghi = cat(h0[n], h1[n]);
      if (n == 0) MFMA_VV0(acc, glo, b1v[n]); else MFMA_VV(acc, glo, b1v[n]);
      if (n + 3 < 32) b1v[n + 3] = lds_ld<u32x4>(up + OU(n + 3) + 2560);
      BLD_A(g.wf[n], (n & 4) ? voff2 : voff, w1so[n >> 3], (n & 3) * 1024);
      SB();
      MFMA_VA(acc, ghi, b0[n]);
      if (n + 3 < 32) { h0[n + 3] = lds_ld<u32x2>(gA + OG(n + 3)); h1[n + 3] = lds_ld<u32x2>(gB + OG(n + 3)); }
      if (n == FC1_SET) {
        asm volatile("s_waitcnt vmcnt(%0)\n\ts_barrier" ::"i"(FC1_SET + 1) : "memory");
        flag_st(rs, fvo, OFF_FC1, tag);
      }
      if (n == 30) pq = poll_issue(rs, OFF_FC1 + pso, voff);
      SB();
    }
    asm volatile("s_nop 11" : "+v"(acc));
#pragma unroll
    for (int q = 0; q < 4; ++q) lds_st(rp + q * 32, f32x4{acc[4 * q], acc[4 * q + 1], acc[4 * q + 2], acc[4 * q + 3]});
    poll_wait<1>(pq, rs, OFF_FC1 + pso, voff, tag);
  }
  bar();
  const int lt0 = 4 * xg + 2 * (bi >> 4), hh = (bi >> 3) & 1, dtp = bi & 7;
  W2F w2f;
  w2f.so = OFF_W2T + (dtp * 56 + 14 * w) * 4096;
#pragma unroll
  for (int q = 0; q < 4; ++q) w2f.vo[q] = opq(voff + ((w + q) & 3) * 1024);
  if (bi < 28) { BLDS4(voff, g.hso, g.hdst + L_HE); BLDS4(voff, g.hso + 4096, g.hdst + L_HE + 4096); }
  conv_out(1);
#pragma unroll
  for (int q = 0; q < 2; ++q) {
    const u32 t = lds0 + (kg0 ^ q) * 8192 + (8 * h + trq) * 64 + trp;
    g.ta[0][q] = opq(t + L_HE); g.ta[1][q] = opq(t + L_HL);
  }
  g.xw = opq(lds0 + L_X + w * 4096 + voff); g.xr = opq(lds0 + L_X + (w ^ 1) * 4096 + voff);
  g.rso = OFF_HT + (lt0 + 32 * (w & 1)) * HT_S + (64 * dtp + 32 * (w >> 1)) * 64 + hh * 32;
  g.rdst = lds0 + L_HR + (4 * (w & 1) + (w >> 1)) * 1024;
  g.rvo = opq((lane >> 1) * 64 + (lane & 1) * 16);
  const u32 fd_vo = ((7 * w) / 2 + min(lane, 3)) * FD_S;
  u32 fda = OFF_FD + lt0 * FD_T;
  asm volatile("" : "+s"(fda));
  const u32 act_so = OFF_ACT + (2 * p1 * 112 + ft) * 1024;
  g.avo = opq(bi < 28 ? (r >> 4) * (56 * 1024) + ((r & 15) + 16 * h) * 16 : FAR);
  f32x16 ac0, ac1;
  G2 e;
  e.aso = OFF_ACT + ((lt0 * 2 + hh) * 56 + 14 * w) * 1024; e.fda = fda; e.fd_vo = fd_vo; e.tag = tag;
  asm volatile("" : "+s"(e.aso));
  if (bi < 28) {
    g.cbr = opq(lds0 + L_CB + (32 * w + 4 * h) * 4); g.fvo = fvo;
    {
      const u32 f = 128 * g14 + (tid & 127);
      const u32 f4 = opq(f * 4), f2 = opq(f * 2);
#pragma unroll
      for (int j = 0; j < 8; ++j) asm volatile("buffer_load_dword %0, %1, %2, %3 offen sc1" : "=v"(g.cp[j]) : "v"(f4), "s"(rs), "s"(OFF_CSP + j * SF * 4) : "memory");
      asm volatile("global_load_ushort %0, %1, %2" : "=v"(g.cp[8]) : "v"(f2), "s"(b1) : "memory");
      g.cbw = opq(lds0 + L_CB + (tid & 127) * 4);
    }
    asm volatile("s_waitcnt vmcnt(9)" ::: "memory");
    g.spw = opq(lds0 + L_SP + w * 256 + r * 8 + h * 4); g.spr = opq(lds0 + L_SP + r * 8);
    g.fda = fda; g.fd_vo = fd_vo;
    g.pso = pso; g.tag = tag; g.fdo = OFF_FD + 2 * p1 * FD_T + g14 * FD_S; g.fdv = tid == 0 ? 0 : OOB;
    g1_tile<0>(g, w2f, ac0, ac0, rs, voff, act_so);
    g1_tile<1>(g, w2f, ac1, ac0, rs, voff, act_so);
    g1_tile<2>(g, w2f, ac0, ac1, rs, voff, act_so);
    g1_tile<3>(g, w2f, ac1, ac0, rs, voff, act_so);
    poll_wait<2 + W2_AFTER>(g.pl, rs, fda, fd_vo, tag);
    e.fl_vo[0] = e.fl_vo[1] = g.fdv; e.fl_so[0] = g.fdo + 32 * FD_T; e.fl_so[1] = g.fdo + 33 * FD_T;
  } else {
    u32x4 dummy = {0u, 0u, 0u, 0u};
    idle_w2<PF0, PF0 + 8>(w2f, dummy, rs, voff);
    asm volatile("s_waitcnt vmcnt(16)\n\ts_barrier" ::: "memory");
    flag_st(rs, fvo, OFF_FC2, tag);
    idle_w2<PF0 + 8, 56>(w2f, dummy, rs, voff);
    w2_range<0, PF0>(w2f, rs);
    poll_wait<0>(poll_issue(rs, OFF_FC2 + pso, voff), rs, OFF_FC2 + pso, voff, tag);
    asm volatile("" ::"v"(dummy));
    bar();
    BLDS1(g.rvo, g.rso, g.rdst); BLDS1(g.rvo, g.rso + HT_S, g.rdst + 2048);
    poll_wait<0>(poll_issue(rs, fda, fd_vo), rs, fda, fd_vo, tag);
    asm volatile("" : "=v"(ac1), "=v"(g.nmu), "=v"(g.rl));
#pragma unroll
    for (int j = 0; j < 8; ++j) asm volatile("" : "=v"(g.cs[j]), "=v"(g.bb[j]));
    e.fl_vo[0] = OOB; e.fl_vo[1] = b == 255 && tid == 0 ? 0 : OOB; e.fl_so[0] = e.fl_so[1] = OFF_GEN;
  }
  bar();
  {
    f32x4 px[4];
#pragma unroll
    for (int q = 0; q < 4; ++q) px[q] = lds_ld<f32x4>(g.xr + q * 1024);
    SB();
    f32x2 gv[8], ev[8];
    u32 ov[8];
#define STAGE(n, x) EACH8(x); g2_pro<(n * DEP) / 8, ((n + 1) * DEP) / 8>(e, rs, voff); SB()
    STAGE(0, gv[j] = g.cs[j] * g.nmu + (f32x2{ac1[2 * j], ac1[2 * j + 1]}));
    STAGE(1, gv[j] += (f32x2{px[j >> 1][2 * (j & 1)], px[j >> 1][2 * (j & 1) + 1]}));
    STAGE(2, gv[j] = gv[j] * g.rl + g.bb[j]);
    STAGE(3, ev[j] = (f32x2{__builtin_amdgcn_exp2f(gv[j][0]), __builtin_amdgcn_exp2f(gv[j][1])}));
    STAGE(4, ev[j] += 1.f);
    STAGE(5, ev[j] = ev[j] * ev[j] + 1.f);
    STAGE(6, ev[j] = (f32x2{__builtin_amdgcn_rcpf(ev[j][0]), __builtin_amdgcn_rcpf(ev[j][1])}));
    STAGE(7, ev[j] = ev[j] * (-2.f * LN2) + LN2);
    EACH8(gv[j] *= ev[j]);
#pragma unroll
    for (int j = 0; j < 8; ++j) ov[j] = pk_bf16(gv[j][0], gv[j][1]);
    st16(rs, g.avo, act_so + 33 * 112 * 1024, u32x4{ov[0], ov[1], ov[2], ov[3]});
    st16(rs, g.avo, act_so + 33 * 112 * 1024 + 512, u32x4{ov[4], ov[5], ov[6], ov[7]});
  }
  const rsrc_t ro = __builtin_amdgcn_make_buffer_rsrc(out, 0, SL * SD * 2, 0x00020000);
  asm volatile("" : "+s"(e.fl_so[0]), "+s"(e.fl_so[1]), "+s"(e.aso));
  e.pl = poll_issue(rs, fda + FD_T, fd_vo);
  {
    const int i = (lane & 15) + 16 * (w & 1) - 8 * (lane >> 4);
#pragma unroll
    for (int j = 0; j < 4; ++j) e.id[j] = i == 2 * j ? 0x3f80u : i == 2 * j + 1 ? 0x3f800000u : 0u;
    const u32 hp = lds0 + L_HR + (w >> 1) * 1024 + (8 * (lane >> 4) + trq) * 32 + (lane & 3) * 8;
#pragma unroll
    for (int m = 0; m < 4; ++m) e.hr[m] = cat(tr_read(hp + m * 2048), tr_read(hp + m * 2048 + 128));
#pragma unroll
    for (int q = 0; q < 4; ++q) e.wp[q] = opq(lds0 + L_G2 + w * 4352 + ((w + q) & 3) * 1088 + (lane >> 4) * 272 + (lane & 15) * 16);
    e.rq = opq(lds0 + L_G2 + ((tid & 15) >> 2) * 1088 + (tid & 3) * 272 + (tid >> 4) * 16);
    e.oo = opq((32 * lt0 + 16 * hh + (tid >> 4)) * 1024 + 128 * dtp + (tid & 15) * 8);
    asm volatile("s_nop 1" : "+v"(e.id));
  }
  G2S<0>::run(e, w2f, rs, ro, voff);
  asm volatile("s_nop 7" : "+v"(e.acc[1][0]), "+v"(e.acc[1][1]), "+v"(e.acc[1][2]), "+v"(e.acc[1][3]));
#pragma unroll
  for (int q = 0; q < 4; ++q) lds_st(e.wp[q] + 17408, e.acc[1][q]);
  bar();
  if (w < 2) {
    const u32 r2 = lds0 + L_G2 + 17408 + ((tid & 7) >> 1) * 1088 + (tid & 1) * 544 + (tid >> 3) * 16;
    const f32x4 v0 = ((lds_ld<f32x4>(r2) + lds_ld<f32x4>(r2 + 4352)) + lds_ld<f32x4>(r2 + 8704)) + lds_ld<f32x4>(r2 + 13056);
    const f32x4 v1 = ((lds_ld<f32x4>(r2 + 272) + lds_ld<f32x4>(r2 + 272 + 4352)) + lds_ld<f32x4>(r2 + 272 + 8704)) + lds_ld<f32x4>(r2 + 272 + 13056);
    __builtin_amdgcn_raw_buffer_store_b128(u32x4{pk_bf16(v0[0], v0[1]), pk_bf16(v0[2], v0[3]), pk_bf16(v1[0], v1[1]), pk_bf16(v1[2], v1[3])}, ro,
                                           (32 * (lt0 + 33) + 16 * hh + (tid >> 3)) * 1024 + 128 * dtp + (tid & 7) * 16, 0, 16);
  }
  asm volatile(".Ls4_end:" ::: "memory");
}

extern "C" void kernel_launch(void* const* d_in, const int* in_sizes, int n_in, void* d_out, int out_size, void* d_ws, size_t ws_size,
                              hipStream_t stream) {
  s4_mlp<<<256, 256, 0, stream>>>((const u16*)d_in[0], (const u16*)d_in[1], (const u16*)d_in[2], (const u16*)d_in[3], (const u16*)d_in[4],
                                  (const u16*)d_in[5], (const u16*)d_in[6], (const u16*)d_in[7], (const u16*)d_in[8], (const u16*)d_in[10],
                                  (const u16*)d_in[11], (const u16*)d_in[12], (u16*)d_out, (char*)d_ws);
}
```
